```python
import torch

def _softmax(x, axis=-1):
    return torch.softmax(x, dim=axis)
def _gelu(x):
    return torch.nn.functional.gelu(x, approximate="tanh")
def _silu(x):
    return torch.nn.functional.silu(x)
def _sigmoid(x):
    return torch.sigmoid(x)
def _layernorm(x, axis=-1, eps=1e-5):
    m = x.mean(dim=axis, keepdim=True)
    v = x.var(dim=axis, keepdim=True, unbiased=False)
    return (x - m) / torch.sqrt(v + eps)
def _scatter_add(tgt, idx, src, axis=0):
    return tgt.clone().index_add_(axis, idx, src)
def _segment_sum(src, seg, n):
    out = torch.zeros((n,) + src.shape[1:], dtype=src.dtype)
    return out.index_add_(0, seg, src)
def _pad(x, pw):
    flat = tuple(v for pair in reversed(pw) for v in pair)
    return torch.nn.functional.pad(x, flat)

kernel_name = 'concat_heads__sliding_local_dilated__linear_attn_block__prompt'

def setup_inputs():

    _w = (lambda a: (lambda t: t.to(torch.bfloat16) if t.is_floating_point() else t)(torch.as_tensor(a)))
    def _A_setup():
        B, S, D = 1, 2048, 512
        return {
            "x": _w((lambda *s: torch.randn(*s, dtype=torch.bfloat16) * 0.2)(B, S, D)),
            "wqkv": _w((lambda *s: torch.randn(*s, dtype=torch.bfloat16) * 0.2)(D, 3 * D)),
            "wo": _w((lambda *s: torch.randn(*s, dtype=torch.bfloat16) * 0.05)(D, D)),
        }
    _A_in = _A_setup()

    def _B_setup():
        B, S, D = 1, 2048, 512
        return {
            "x": _w((lambda *s: torch.randn(*s, dtype=torch.bfloat16) * 0.05)(B, S, D)),
            "wqkv": _w((lambda *s: torch.randn(*s, dtype=torch.bfloat16) * 1.6)(D, 3 * D)),
            "wo": _w((lambda *s: torch.randn(*s, dtype=torch.bfloat16) * 0.8)(D, D)),
        }
    _B_in = _B_setup()

    return {
        "A_x": _A_in["x"],
        "A_wqkv": _A_in["wqkv"],
        "A_wo": _A_in["wo"],
        "B_x": _B_in["x"],
        "B_wqkv": _B_in["wqkv"],
        "B_wo": _B_in["wo"],
    }

def reference(A_x, A_wqkv, A_wo, B_x, B_wqkv, B_wo):
    def _A_ref(x, wqkv, wo):
        B, S, D = x.shape
        H = 32; Dh = D // H
        W, dil = 8, 2
        qkv = (lambda x, *d: x.permute(*d))((x @ wqkv).reshape(B, S, 3, H, Dh), 2, 0, 3, 1, 4)
        q, k, v = qkv[0], qkv[1], qkv[2]
        scale = Dh ** -0.5
        pos = torch.arange(S)
        off = torch.arange(W) * dil
        idx = pos[:, None] - off[None, :]
        mask = idx >= 0
        idx_c = torch.where(mask, idx, 0).reshape(-1)
        kg = k[:, :, idx_c, :].reshape(B, H, S, W, Dh)
        vg = v[:, :, idx_c, :].reshape(B, H, S, W, Dh)
        logits = torch.einsum("bhsd,bhswd->bhsw", q, kg) * scale
        logits = torch.where(mask[None, None, :, :], logits, -1e9)
        attn = _softmax(logits, axis=-1)
        o = torch.einsum("bhsw,bhswd->bhsd", attn, vg)
        return (lambda x, *d: x.permute(*d))(o, 0, 2, 1, 3).reshape(B, S, D) @ wo

    def _B_ref(x, wqkv, wo):
        B, S, D = x.shape
        H = 32; Dh = D // H
        qkv = (lambda x, *d: x.permute(*d))((x @ wqkv).reshape(B, S, 3, H, Dh), 2, 0, 3, 1, 4)
        q, k, v = qkv[0], qkv[1], qkv[2]
        phi = lambda t: torch.where(t > 0, t + 1, torch.exp(t)) + 1e-6
        pq, pk = phi(q), phi(k)
        kv = torch.einsum("bhsd,bhse->bhde", pk, v)
        z = torch.einsum("bhsd->bhd", pk)
        num = torch.einsum("bhqd,bhde->bhqe", pq, kv)
        den = torch.einsum("bhqd,bhd->bhq", pq, z)[..., None] + 1e-6
        o = num / den
        return (lambda x, *d: x.permute(*d))(o, 0, 2, 1, 3).reshape(B, S, D) @ wo

    a = _A_ref(A_x, A_wqkv, A_wo)
    b = _B_ref(B_x, B_wqkv, B_wo)
    return torch.concatenate([a, b], axis=-1)
```

```cpp
#include <hip/hip_runtime.h>
#include <hip/hip_bf16.h>
typedef __hip_bfloat16 bf16;
constexpr int S = 2048, D = 512, H = 32, DH = 16;

__device__ inline float b2f(bf16 v) { return __bfloat162float(v); }
__device__ inline bf16 f2b(float v) { return __float2bfloat16(v); }

__global__ void gemm_naive(const bf16* A, const bf16* B, bf16* C, int M, int N, int K, int ldc) {
    int n = blockIdx.x * 64 + (threadIdx.x & 63);
    int m = blockIdx.y * 4 + (threadIdx.x >> 6);
    float acc = 0.f;
    for (int k = 0; k < K; ++k) acc += b2f(A[m * K + k]) * b2f(B[k * N + n]);
    C[m * ldc + n] = f2b(acc);
}

__global__ void attnA(const bf16* qkv, bf16* o) {
    int idx = blockIdx.x * blockDim.x + threadIdx.x;
    int s = idx / H, h = idx % H;
    float l[8]; float mx = -1e30f;
#pragma unroll
    for (int w = 0; w < 8; ++w) {
        int t = s - 2 * w;
        float a = -1e9f;
        if (t >= 0) {
            a = 0.f;
            for (int d = 0; d < DH; ++d) a += b2f(qkv[s * 1536 + h * 16 + d]) * b2f(qkv[t * 1536 + 512 + h * 16 + d]);
            a *= 0.25f;
        }
        l[w] = a; mx = fmaxf(mx, a);
    }
    float sum = 0.f;
#pragma unroll
    for (int w = 0; w < 8; ++w) { l[w] = __expf(l[w] - mx); sum += l[w]; }
    for (int d = 0; d < DH; ++d) {
        float a = 0.f;
#pragma unroll
        for (int w = 0; w < 8; ++w) { int t = s - 2 * w; if (t < 0) t = 0; a += l[w] * b2f(qkv[t * 1536 + 1024 + h * 16 + d]); }
        o[s * 512 + h * 16 + d] = f2b(a / sum);
    }
}
__device__ inline float phi(float t) { return (t > 0.f ? t + 1.f : __expf(t)) + 1e-6f; }
__global__ void kvB(const bf16* qkv, float* kv, float* z) {
    int h = blockIdx.x, d = threadIdx.x >> 4, e = threadIdx.x & 15;
    float a = 0.f, zz = 0.f;
    for (int s = 0; s < S; ++s) {
        float pk = phi(b2f(qkv[s * 1536 + 512 + h * 16 + d]));
        a += pk * b2f(qkv[s * 1536 + 1024 + h * 16 + e]);
        zz += pk;
    }
    kv[h * 256 + d * 16 + e] = a;
    if (e == 0) z[h * 16 + d] = zz;
}
__global__ void oB(const bf16* qkv, const float* kv, const float* z, bf16* o) {
    int idx = blockIdx.x * blockDim.x + threadIdx.x;
    int s = idx / 512, c = idx % 512, h = c / 16, e = c % 16;
    float num = 0.f, den = 0.f;
    for (int d = 0; d < 16; ++d) {
        float pq = phi(b2f(qkv[s * 1536 + h * 16 + d]));
        num += pq * kv[h * 256 + d * 16 + e];
        den += pq * z[h * 16 + d];
    }
    o[s * 512 + c] = f2b(num / (den + 1e-6f));
}

extern "C" void kernel_launch(void* const* d_in, const int* in_sizes, int n_in,
                              void* d_out, int out_size, void* d_ws, size_t ws_size,
                              hipStream_t stream) {
    const bf16* Ax = (const bf16*)d_in[0]; const bf16* Aw = (const bf16*)d_in[1]; const bf16* Awo = (const bf16*)d_in[2];
    const bf16* Bx = (const bf16*)d_in[3]; const bf16* Bw = (const bf16*)d_in[4]; const bf16* Bwo = (const bf16*)d_in[5];
    bf16* out = (bf16*)d_out;
    char* ws = (char*)d_ws;
    bf16* qkvA = (bf16*)ws; bf16* qkvB = (bf16*)(ws + (8 << 20));
    bf16* oA = (bf16*)(ws + (16 << 20)); bf16* oBp = (bf16*)(ws + (20 << 20));
    float* kv = (float*)(ws + (24 << 20)); float* z = (float*)(ws + (25 << 20));
    gemm_naive<<<dim3(1536 / 64, S / 4), 256, 0, stream>>>(Ax, Aw, qkvA, S, 1536, 512, 1536);
    gemm_naive<<<dim3(1536 / 64, S / 4), 256, 0, stream>>>(Bx, Bw, qkvB, S, 1536, 512, 1536);
    attnA<<<S * H / 256, 256, 0, stream>>>(qkvA, oA);
    kvB<<<H, 256, 0, stream>>>(qkvB, kv, z);
    oB<<<S * 512 / 256, 256, 0, stream>>>(qkvB, kv, z, oBp);
    gemm_naive<<<dim3(512 / 64, S / 4), 256, 0, stream>>>(oA, Awo, out, S, 512, 512, 1024);
    gemm_naive<<<dim3(512 / 64, S / 4), 256, 0, stream>>>(oBp, Bwo, out + 512, S, 512, 512, 1024);
}
```

```cpp
#include <hip/hip_runtime.h>
#include <hip/hip_bf16.h>
#include <stdint.h>

#define STAMPS 0

typedef short bf16x8 __attribute__((ext_vector_type(8)));
typedef short bf16x4 __attribute__((ext_vector_type(4)));
typedef float f32x4 __attribute__((ext_vector_type(4)));
typedef float f32x2 __attribute__((ext_vector_type(2)));
typedef __bf16 bf16v2 __attribute__((ext_vector_type(2)));
typedef unsigned u32x4 __attribute__((ext_vector_type(4)));
typedef unsigned u32x2 __attribute__((ext_vector_type(2)));
typedef unsigned long long u64;
#define LAS __attribute__((address_space(3)))
#define GAS __attribute__((address_space(1)))
typedef LAS const char* lcp;
typedef GAS u64 gu64;

constexpr int LDS_BYTES = 163840;
constexpr int A_SLOT = 40960, A_HALO = 122880, A_HROW = 1088;
constexpr int B_SLOT = 32768;

constexpr unsigned WS_EPOCH = 0;
constexpr unsigned WS_FO = 8192;
constexpr unsigned WS_FKV = 8192 + 65536;
constexpr unsigned FLAG_STRIDE = 128;
constexpr unsigned WS_KV = 262144;
constexpr unsigned WS_Z = WS_KV + 524288;
constexpr unsigned WS_O = 1048576;
constexpr unsigned WS_END = WS_O + 4194304;
constexpr unsigned WS_STAMP = 8388608;
constexpr unsigned SPIN_CAP = 1u << 18;

#define RLX_AGENT __ATOMIC_RELAXED, __HIP_MEMORY_SCOPE_AGENT
#define SBAR() __builtin_amdgcn_sched_barrier(0)
#define PIN(x) asm volatile("" : "+v"(x))
#define WAIT_BAR(N) asm volatile("s_waitcnt vmcnt(" #N ") lgkmcnt(0)\n\ts_barrier" ::: "memory")
#define DRAIN() asm volatile("s_waitcnt vmcnt(0)" ::: "memory")
#define MF32(a, b, c) __builtin_amdgcn_mfma_f32_16x16x32_bf16(a, b, c, 0, 0, 0)
#define MF16(a, b, c) __builtin_amdgcn_mfma_f32_16x16x16bf16_1k(a, b, c, 0, 0, 0)

#define STAMP(k) do {} while (0)

__device__ __forceinline__ void glds16(unsigned voff, const void* sbase, unsigned lds_addr) {
    unsigned keep;
    asm volatile("s_mov_b32 %0, m0\n\ts_mov_b32 m0, %3\n\ts_nop 0\n\tglobal_load_lds_dwordx4 %1, %2\n\ts_mov_b32 m0, %0"
                 : "=&s"(keep) : "v"(voff), "s"(sbase), "s"(lds_addr) : "memory");
}
__device__ __forceinline__ bf16x8 lds128(lcp p) { return *(const LAS bf16x8*)p; }
__device__ __forceinline__ bf16x4 ldstr(lcp p) { return __builtin_amdgcn_ds_read_tr16_b64_v4i16((LAS bf16x4*)p); }
__device__ __forceinline__ bf16x8 ldsfragT(lcp p) {
    bf16x4 a = ldstr(p), b = ldstr(p + 512);
    return __builtin_shufflevector(a, b, 0, 1, 2, 3, 4, 5, 6, 7);
}
__device__ __forceinline__ unsigned pk2(float a, float b) {
    f32x2 v = {a, b};
    return __builtin_bit_cast(unsigned, __builtin_convertvector(v, bf16v2));
}
__device__ __forceinline__ u32x2 pk4(f32x4 v) { return u32x2{pk2(v[0], v[1]), pk2(v[2], v[3])}; }
__device__ __forceinline__ bf16x4 as4(u32x2 v) { return __builtin_bit_cast(bf16x4, v); }
__device__ __forceinline__ bf16x8 as8(u32x2 a, u32x2 b) { return __builtin_bit_cast(bf16x8, u32x4{a[0], a[1], b[0], b[1]}); }
__device__ __forceinline__ u32x4 swap16(u32x2 a, u32x2 b) {
    auto x = __builtin_amdgcn_permlane16_swap(a[0], b[0], false, false);
    auto y = __builtin_amdgcn_permlane16_swap(a[1], b[1], false, false);
    return u32x4{x[0], y[0], x[1], y[1]};
}
__device__ __forceinline__ float xmax(float m) {
    auto a = __builtin_amdgcn_permlane16_swap(__float_as_uint(m), __float_as_uint(m), false, false);
    m = __builtin_fmaxf(__uint_as_float(a[0]), __uint_as_float(a[1]));
    auto b = __builtin_amdgcn_permlane32_swap(__float_as_uint(m), __float_as_uint(m), false, false);
    return __builtin_fmaxf(__uint_as_float(b[0]), __uint_as_float(b[1]));
}
__device__ __forceinline__ float phi(float t) {
    float e = __builtin_amdgcn_exp2f(t * 1.4426950408889634f);
    return (t > 0.f ? t + 1.f : e) + 1e-6f;
}
__device__ __forceinline__ f32x4 phi4(f32x4 v) { return f32x4{phi(v[0]), phi(v[1]), phi(v[2]), phi(v[3])}; }
template <int CTRL> __device__ __forceinline__ float dpp_add(float v) {
    return v + __int_as_float(__builtin_amdgcn_update_dpp(0, __float_as_int(v), CTRL, 0xf, 0xf, false));
}
__device__ __forceinline__ float row_sum(float v) {
    v = dpp_add<0x128>(v); v = dpp_add<0x124>(v); v = dpp_add<0x122>(v); v = dpp_add<0x121>(v);
    return v;
}

__device__ __forceinline__ void poll_flags(const char* ws, unsigned off, int n, u64 tag, int lane) {
    gu64* p = (gu64*)(ws + off + (unsigned)(lane < n ? lane : 0) * FLAG_STRIDE);
    for (unsigned spins = 0; spins < SPIN_CAP; ++spins) {
        u64 v = __hip_atomic_load(p, RLX_AGENT);
        if (__all(v == tag)) break;
        __builtin_amdgcn_s_sleep(1);
    }
    __builtin_amdgcn_fence(__ATOMIC_ACQUIRE, "wavefront");
}

__global__ void __launch_bounds__(512, 2)
fused_kernel(const char* __restrict__ xA, const char* __restrict__ wA, const char* __restrict__ woA,
             const char* __restrict__ xB, const char* __restrict__ wB, const char* __restrict__ woB,
             char* __restrict__ out, char* ws) {
    __shared__ __attribute__((aligned(16))) char smem[LDS_BYTES];
    const int tid = threadIdx.x, lane = tid & 63;
    const int w = __builtin_amdgcn_readfirstlane(tid >> 6);
    const int b = blockIdx.x, xcd = b & 7, idx = b >> 3;
    const int br = xcd >> 2, r = (xcd & 3) * 4 + (idx >> 3), g = idx & 7;
    const int s0 = r * 128;
    const int i16 = lane & 15, G = lane >> 4, u = w & 3, hf = w >> 2;
    const unsigned lds0 = (unsigned)(uintptr_t)smem;
    const lcp L = (lcp)smem;
    STAMP(0);

    const char* x = br ? xB : xA;
    const char* wq = br ? wB : wA;
    const char* wo = br ? woB : woA;

    unsigned xoff[2];
#pragma unroll
    for (int e = 0; e < 2; ++e) {
        const int tok = s0 + (w >> 2) + 32 * (w & 3) + 2 * (8 * e + (lane >> 3));
        xoff[e] = (unsigned)tok * 1024u + (unsigned)(((lane & 7) ^ (4 * e + G)) * 16);
    }
    const int pc = lane & 7;
    const unsigned woff = (unsigned)(8 * w + (lane >> 3)) * 3072u + (unsigned)(g * 128) +
                          (unsigned)((((pc >> 1) ^ ((G & 1) | ((w & 1) << 1))) * 32) + (pc & 1) * 16);
    const unsigned crow = (unsigned)(32 * w + (lane >> 3));
    const unsigned cswz0 = (unsigned)(g * 128) + (unsigned)((((pc >> 1) ^ (G & 1)) * 32) + (pc & 1) * 16);
    const unsigned cswz1 = cswz0 ^ 64u;

    const int q4 = i16 >> 2, p4 = lane & 3;
    const int ftr = ((q4 >> 1) & 1) | ((G & 1) << 1);
    const unsigned trrow = (unsigned)((8 * G + q4) * 128 + p4 * 8);
    const unsigned xrd0 = (unsigned)(i16 * 128 + ((G ^ (i16 >> 1)) * 16));

    const __amdgpu_buffer_rsrc_t rs = __builtin_amdgcn_make_buffer_rsrc(ws, 0, WS_END, 0x00020000);
    const unsigned ep_off = WS_EPOCH + (unsigned)(br ? 16 : r) * 256u;
    u64 epoch;
    unsigned wo_lo, wo_hi;

    if (br == 0) {
#define A_STAGE(t) do { const unsigned d_ = lds0 + ((t) % 3) * A_SLOT; \
        glds16(xoff[0], x + (t) * 128, d_ + w * 2048); glds16(xoff[1], x + (t) * 128, d_ + w * 2048 + 1024); \
        glds16(woff, wq + (t) * 196608, d_ + 16384 + w * 1024); \
        glds16(woff, wq + (t) * 196608 + 1024, d_ + 24576 + w * 1024); \
        glds16(woff, wq + (t) * 196608 + 2048, d_ + 32768 + w * 1024); } while (0)
        A_STAGE(0);
#pragma unroll
        for (int e = 0; e < 2; ++e) {
            const int eta = 2 * w + e;
            int tok = s0 - 16 + (eta >> 3) + 2 * (eta & 7);
            tok = tok < 0 ? 0 : tok;
            const unsigned hoff = (unsigned)tok * 1024u + (unsigned)((lane ^ ((eta >> 3) << 1)) * 16);
            glds16(hoff, x, lds0 + A_HALO + eta * A_HROW);
        }
        A_STAGE(1);
        A_STAGE(2);
        asm volatile("s_load_dwordx2 %0, %1, %2\n\ts_waitcnt lgkmcnt(0)" : "=&s"(epoch) : "s"(ws), "s"(ep_off) : "memory");

        const int eta_r = i16 ^ (hf ? 0 : 8);
        const lcp hrd = L + A_HALO + eta_r * A_HROW + ((G ^ ((eta_r >> 3) << 1)) * 16);
        const lcp xr0 = L + xrd0 + hf * 8192, xr1 = L + (xrd0 ^ 64u) + hf * 8192;
        const lcp wr = L + 16384 + trrow + ((u ^ ftr) * 32);

        f32x4 aq[4], ak[5], av[5];
#pragma unroll
        for (int j = 0; j < 4; ++j) aq[j] = f32x4{0.f, 0.f, 0.f, 0.f};
#pragma unroll
        for (int j = 0; j < 5; ++j) { ak[j] = f32x4{0.f, 0.f, 0.f, 0.f}; av[j] = f32x4{0.f, 0.f, 0.f, 0.f}; }
        bf16x8 xf[2][5], wf[2][3];
#define A_READ(kk) do { const int so_ = (((kk) >> 1) % 3) * A_SLOT; const lcp xb_ = ((kk) & 1) ? xr1 : xr0; \
        _Pragma("unroll") for (int s_ = 0; s_ < 3; ++s_) wf[(kk) & 1][s_] = ldsfragT(wr + so_ + s_ * 8192 + ((kk) & 1) * 4096); \
        _Pragma("unroll") for (int j_ = 0; j_ < 4; ++j_) xf[(kk) & 1][j_ + 1] = lds128(xb_ + so_ + j_ * 2048); \
        xf[(kk) & 1][0] = lds128(hrd + (kk) * 64); } while (0)
#define A_MMA(kk) do { \
        _Pragma("unroll") for (int j_ = 0; j_ < 4; ++j_) { \
            aq[j_] = MF32(wf[(kk) & 1][0], xf[(kk) & 1][j_ + 1], aq[j_]); \
            ak[j_ + 1] = MF32(wf[(kk) & 1][1], xf[(kk) & 1][j_ + 1], ak[j_ + 1]); \
            av[j_ + 1] = MF32(xf[(kk) & 1][j_ + 1], wf[(kk) & 1][2], av[j_ + 1]); } \
        ak[0] = MF32(wf[(kk) & 1][1], xf[(kk) & 1][0], ak[0]); \
        av[0] = MF32(xf[(kk) & 1][0], wf[(kk) & 1][2], av[0]); } while (0)
#define CHUNK(src, pitch, dst) do { _Pragma("unroll") for (int n_ = 0; n_ < 4; ++n_) \
        glds16((crow + 8 * n_) * (pitch) + ((n_ & 1) ? cswz1 : cswz0), (src), lds0 + (dst) + w * 4096 + n_ * 1024); } while (0)

        WAIT_BAR(10);
        STAMP(1);
        A_READ(0);
#pragma unroll
        for (int t = 0; t < 8; ++t) {
            A_READ(2 * t + 1);
            SBAR();
            A_MMA(2 * t);
            SBAR();
            if (t < 7) {
                if (t < 6) WAIT_BAR(5); else WAIT_BAR(4);
                if (t < 5) A_STAGE(t + 3);
                else if (t == 5) CHUNK(wo + 262144, 1024u, 2 * A_SLOT);
                else CHUNK(wo, 1024u, 0);
                A_READ(2 * t + 2);
                SBAR();
            }
            A_MMA(2 * t + 1);
            SBAR();
        }
        wo_lo = 0; wo_hi = 2 * A_SLOT;
        STAMP(2);

        f32x4 ccur, cprev, cnone;
        unsigned mcur[2];
#pragma unroll
        for (int e = 0; e < 4; ++e) {
            const int key = 4 * G + e;
            ccur[e] = (key <= i16 && i16 - key <= 7) ? 0.f : -1e30f;
            cprev[e] = (key >= i16 + 9) ? 0.f : -1e30f;
            cnone[e] = (r == 0) ? -1e30f : cprev[e];
        }
#pragma unroll
        for (int e = 0; e < 2; ++e)
            mcur[e] = ((4 * G + 2 * e <= i16) ? 0xffffu : 0u) | ((4 * G + 2 * e + 1 <= i16) ? 0xffff0000u : 0u);
        u32x2 kb[5], vb[5], ob[4];
#pragma unroll
        for (int j = 0; j < 5; ++j) { kb[j] = pk4(ak[j]); vb[j] = pk4(av[j]); }
        const bf16x4 ones = {0x3f80, 0x3f80, 0x3f80, 0x3f80};
        const float cs = 0.25f * 1.4426950408889634f;
#pragma unroll
        for (int j = 0; j < 4; ++j) {
            const bf16x4 qb = as4(pk4(aq[j]));
            const f32x4 sc = MF16(as4(kb[j + 1]), qb, ccur);
            const f32x4 sp = MF16(as4(kb[j]), qb, j == 0 ? cnone : cprev);
            f32x4 s;
#pragma unroll
            for (int e = 0; e < 4; ++e) s[e] = __builtin_fmaxf(sc[e], sp[e]);
            const float m = xmax(__builtin_fmaxf(__builtin_fmaxf(s[0], s[1]), __builtin_fmaxf(s[2], s[3])));
            const float nm = -m * cs;
            f32x4 p;
#pragma unroll
            for (int e = 0; e < 4; ++e) p[e] = __builtin_amdgcn_exp2f(__builtin_fmaf(s[e], cs, nm));
            const u32x2 pb = pk4(p);
            const u32x2 pcur = {pb[0] & mcur[0], pb[1] & mcur[1]}, pprev = {pb[0] & ~mcur[0], pb[1] & ~mcur[1]};
            const f32x4 sum = MF16(ones, as4(pb), (f32x4{0.f, 0.f, 0.f, 0.f}));
            const f32x4 o = MF32(as8(vb[j], vb[j + 1]), as8(pprev, pcur), (f32x4{0.f, 0.f, 0.f, 0.f}));
            const float inv = __builtin_amdgcn_rcpf(sum[0]);
            ob[j] = pk4(f32x4{o[0] * inv, o[1] * inv, o[2] * inv, o[3] * inv});
        }
        const int h = 4 * g + u;
#pragma unroll
        for (int jp = 0; jp < 2; ++jp) {
            const u32x4 v = swap16(ob[2 * jp], ob[2 * jp + 1]);
            const int T = 4 * hf + 2 * jp + (G & 1);
            const unsigned off = WS_O + (unsigned)((((r * 8 + T) * 16 + (h >> 1)) * 4 + 2 * (h & 1) + (G >> 1)) * 256 + i16 * 16);
            __builtin_amdgcn_raw_buffer_store_b128(v, rs, off, 0, 16);
        }
    } else {
#define B_STAGE(t) do { const unsigned d_ = lds0 + ((t) % 5) * B_SLOT; \
        glds16(xoff[0], x + (t) * 128, d_ + w * 2048); glds16(xoff[1], x + (t) * 128, d_ + w * 2048 + 1024); \
        glds16(woff, wq + (t) * 196608 + 1024, d_ + 16384 + w * 1024); \
        glds16(woff, wq + (t) * 196608 + 2048, d_ + 24576 + w * 1024); } while (0)
        B_STAGE(0); B_STAGE(1); B_STAGE(2); B_STAGE(3); B_STAGE(4);
        asm volatile("s_load_dwordx2 %0, %1, %2\n\ts_waitcnt lgkmcnt(0)" : "=&s"(epoch) : "s"(ws), "s"(ep_off) : "memory");

        unsigned xa[4];
#pragma unroll
        for (int j = 0; j < 4; ++j) xa[j] = xrd0 + (unsigned)((4 * hf + (j ^ u)) * 2048);
        const lcp wr = L + 16384 + trrow + ((u ^ ftr) * 32);
        f32x4 ak[4], av[4];
#pragma unroll
        for (int j = 0; j < 4; ++j) { ak[j] = f32x4{0.f, 0.f, 0.f, 0.f}; av[j] = f32x4{0.f, 0.f, 0.f, 0.f}; }
        bf16x8 xs[16], xf[2][3], wf[2][2];
#define B_READ(kk) do { const int so_ = (((kk) >> 1) % 5) * B_SLOT; const unsigned xx_ = ((kk) & 1) ? 64u : 0u; \
        _Pragma("unroll") for (int s_ = 0; s_ < 2; ++s_) wf[(kk) & 1][s_] = ldsfragT(wr + so_ + s_ * 8192 + ((kk) & 1) * 4096); \
        xs[kk] = lds128(L + (xa[0] ^ xx_) + so_); \
        _Pragma("unroll") for (int j_ = 1; j_ < 4; ++j_) xf[(kk) & 1][j_ - 1] = lds128(L + (xa[j_] ^ xx_) + so_); } while (0)
#define B_MMA(kk) do { \
        ak[0] = MF32(xs[kk], wf[(kk) & 1][0], ak[0]); av[0] = MF32(xs[kk], wf[(kk) & 1][1], av[0]); \
        _Pragma("unroll") for (int j_ = 1; j_ < 4; ++j_) { \
            ak[j_] = MF32(xf[(kk) & 1][j_ - 1], wf[(kk) & 1][0], ak[j_]); \
            av[j_] = MF32(xf[(kk) & 1][j_ - 1], wf[(kk) & 1][1], av[j_]); } } while (0)

        WAIT_BAR(16);
        STAMP(1);
        B_READ(0);
#pragma unroll
        for (int t = 0; t < 8; ++t) {
            B_READ(2 * t + 1);
            SBAR();
            B_MMA(2 * t);
            SBAR();
            if (t < 7) {
                WAIT_BAR(12);
                if (t < 3) B_STAGE(t + 5);
                else if (t == 3) CHUNK(wq, 3072u, 3 * B_SLOT);
                else if (t == 4) CHUNK(wq + 786432, 3072u, 4 * B_SLOT);
                else if (t == 5) CHUNK(wo, 1024u, 0);
                B_READ(2 * t + 2);
                SBAR();
            }
            B_MMA(2 * t + 1);
            SBAR();
        }
        STAMP(2);
        wo_lo = 0; wo_hi = 2 * B_SLOT;

        const bf16x8 ones8 = {0x3f80, 0x3f80, 0x3f80, 0x3f80, 0x3f80, 0x3f80, 0x3f80, 0x3f80};
        f32x4 kv = {0.f, 0.f, 0.f, 0.f}, zz = {0.f, 0.f, 0.f, 0.f};
#pragma unroll
        for (int jp = 0; jp < 2; ++jp) {
            const bf16x8 pkf = as8(pk4(phi4(ak[2 * jp])), pk4(phi4(ak[2 * jp + 1])));
            const bf16x8 vf = as8(pk4(av[2 * jp]), pk4(av[2 * jp + 1]));
            kv = MF32(pkf, vf, kv);
            zz = MF32(pkf, ones8, zz);
        }
        LAS char* scr = (LAS char*)smem + B_SLOT;
        if (hf) {
            *(LAS f32x4*)(scr + u * 2048 + lane * 16) = kv;
            *(LAS f32x4*)(scr + u * 2048 + 1024 + lane * 16) = zz;
        }
        if (tid == 0) *(LAS unsigned*)(scr + 8192) = 0u;
        WAIT_BAR(4);
        const int h = 4 * g + u;
        const u64 tag = epoch + 1;
        if (!hf) {
            kv += *(const LAS f32x4*)(scr + u * 2048 + lane * 16);
            zz += *(const LAS f32x4*)(scr + u * 2048 + 1024 + lane * 16);
            __builtin_amdgcn_raw_buffer_store_b128(__builtin_bit_cast(u32x4, kv), rs, WS_KV + (unsigned)((h * 16 + r) * 1024 + lane * 16), 0, 16);
            if (i16 == 0)
                __builtin_amdgcn_raw_buffer_store_b128(__builtin_bit_cast(u32x4, zz), rs, WS_Z + (unsigned)((h * 16 + r) * 128 + G * 16), 0, 16);
            DRAIN();
            if (lane == 0) {
                const unsigned n = __hip_atomic_fetch_add((LAS unsigned*)(scr + 8192), 1u, __ATOMIC_RELAXED, __HIP_MEMORY_SCOPE_WORKGROUP);
                if (n == 3) __hip_atomic_store((gu64*)(ws + WS_FKV + (unsigned)(g * 16 + r) * FLAG_STRIDE), tag, RLX_AGENT);
            }
        }
        STAMP(3);
        CHUNK(wo + 262144, 1024u, 2 * B_SLOT);

        f32x4 aq[4];
#pragma unroll
        for (int c = 0; c < 4; ++c) aq[c] = f32x4{0.f, 0.f, 0.f, 0.f};
#pragma unroll
        for (int kk = 0; kk < 16; ++kk) {
#pragma unroll
            for (int c = 0; c < 4; ++c) {
                const bf16x8 f = ldsfragT(L + 3 * B_SLOT + trrow + ((c ^ ftr) * 32) + kk * 4096);
                aq[c] = MF32(f, xs[kk], aq[c]);
            }
        }
        u32x2 pq[4];
#pragma unroll
        for (int c = 0; c < 4; ++c) pq[c] = pk4(phi4(aq[c]));
        STAMP(4);

        if (w == 0) poll_flags(ws, WS_FKV + (unsigned)(g * 16) * FLAG_STRIDE, 16, tag, lane);
        asm volatile("s_barrier" ::: "memory");
        STAMP(5);
        u32x4 pv[8];
#pragma unroll
        for (int j = 0; j < 8; ++j)
            pv[j] = __builtin_amdgcn_raw_buffer_load_b128(rs, WS_KV + (unsigned)((h * 16 + 8 * hf + j) * 1024 + lane * 16), 0, 16);
        const u32x4 zv = __builtin_amdgcn_raw_buffer_load_b128(rs, WS_Z + (unsigned)((h * 16 + i16) * 128 + G * 16), 0, 16);
        f32x4 ks = __builtin_bit_cast(f32x4, pv[0]);
#pragma unroll
        for (int j = 1; j < 8; ++j) ks += __builtin_bit_cast(f32x4, pv[j]);
        LAS char* scr2 = scr + 16384;
        *(LAS f32x4*)(scr2 + w * 1024 + lane * 16) = ks;
        if (!hf) {
            const f32x4 zf = __builtin_bit_cast(f32x4, zv);
            const f32x4 zs = {row_sum(zf[0]), row_sum(zf[1]), row_sum(zf[2]), row_sum(zf[3])};
            *(LAS u32x2*)(scr2 + 8192 + u * 512 + lane * 8) = pk4(zs);
        }
        asm volatile("s_waitcnt lgkmcnt(0)\n\ts_barrier" ::: "memory");
        u32x2 ob[4];
#pragma unroll
        for (int c = 0; c < 4; ++c) {
            const f32x4 k0 = *(const LAS f32x4*)(scr2 + c * 1024 + lane * 16);
            const f32x4 k1 = *(const LAS f32x4*)(scr2 + 4096 + c * 1024 + lane * 16);
            const u32x2 zb = *(const LAS u32x2*)(scr2 + 8192 + c * 512 + lane * 8);
            const f32x4 num = MF16(as4(pk4(k0 + k1)), as4(pq[c]), (f32x4{0.f, 0.f, 0.f, 0.f}));
            const f32x4 den = MF16(as4(zb), as4(pq[c]), (f32x4{1e-6f, 1e-6f, 1e-6f, 1e-6f}));
            const float inv = __builtin_amdgcn_rcpf(den[0]);
            ob[c] = pk4(f32x4{num[0] * inv, num[1] * inv, num[2] * inv, num[3] * inv});
        }
#pragma unroll
        for (int cp = 0; cp < 2; ++cp) {
            const u32x4 v = swap16(ob[2 * cp], ob[2 * cp + 1]);
            const unsigned off = WS_O + (unsigned)(((((16 + r) * 8 + w) * 16 + 2 * g + cp) * 4 + 2 * (G & 1) + (G >> 1)) * 256 + i16 * 16);
            __builtin_amdgcn_raw_buffer_store_b128(v, rs, off, 0, 16);
        }
    }

    const u64 tag = epoch + 1;
    const unsigned fo = WS_FO + (unsigned)((br * 16 + r) * 8) * FLAG_STRIDE;
    STAMP(6);
    WAIT_BAR(0);
    if (w == 0) {
        if (lane == 0) __hip_atomic_store((gu64*)(ws + fo + (unsigned)g * FLAG_STRIDE), tag, RLX_AGENT);
        STAMP(7);
        poll_flags(ws, fo, 8, tag, lane);
        if (lane == 0 && g == 0 && (br == 0 || r == 0)) __hip_atomic_store((gu64*)(ws + ep_off), tag, RLX_AGENT);
    }
    asm volatile("s_barrier" ::: "memory");
    STAMP(8);
    u32x4 of[16];
#pragma unroll
    for (int s = 0; s < 16; ++s)
        of[s] = __builtin_amdgcn_raw_buffer_load_b128(rs, WS_O + (unsigned)((((br * 16 + r) * 8 + w) * 16 + s) * 1024 + lane * 16), 0, 16);
    SBAR();
    bf16x8 wpre[8][4];
#pragma unroll
    for (int s = 0; s < 8; ++s)
#pragma unroll
        for (int c = 0; c < 4; ++c) wpre[s][c] = ldsfragT(L + wo_lo + trrow + ((c ^ ftr) * 32) + s * 4096);
    SBAR();
    f32x4 acc[4];
#pragma unroll
    for (int c = 0; c < 4; ++c) acc[c] = f32x4{0.f, 0.f, 0.f, 0.f};
#pragma unroll
    for (int s = 0; s < 8; ++s)
#pragma unroll
        for (int c = 0; c < 4; ++c) {
            acc[c] = MF32(wpre[s][c], __builtin_bit_cast(bf16x8, of[s]), acc[c]);
            wpre[s][c] = ldsfragT(L + wo_hi + trrow + ((c ^ ftr) * 32) + s * 4096);
        }
#pragma unroll
    for (int s = 0; s < 8; ++s)
#pragma unroll
        for (int c = 0; c < 4; ++c) acc[c] = MF32(wpre[s][c], __builtin_bit_cast(bf16x8, of[8 + s]), acc[c]);
    STAMP(9);
    const int tok = s0 + (w >> 2) + 32 * (w & 3) + 2 * i16;
    char* orow = out + (size_t)tok * 2048 + br * 1024 + g * 128 + (G & 1) * 32 + (G >> 1) * 16;
#pragma unroll
    for (int cp = 0; cp < 2; ++cp) {
        const u32x4 v = swap16(pk4(acc[2 * cp]), pk4(acc[2 * cp + 1]));
        *(u32x4*)(orow + cp * 64) = v;
    }
    STAMP(10);
}

extern "C" void kernel_launch(void* const* d_in, const int* in_sizes, int n_in,
                              void* d_out, int out_size, void* d_ws, size_t ws_size,
                              hipStream_t stream) {
    fused_kernel<<<256, 512, 0, stream>>>((const char*)d_in[0], (const char*)d_in[1], (const char*)d_in[2],
                                                  (const char*)d_in[3], (const char*)d_in[4], (const char*)d_in[5],
                                                  (char*)d_out, (char*)d_ws);
}
```

```cpp
#define PROBE 99
#define PROBE_BR 1
#include <hip/hip_runtime.h>
#include <hip/hip_bf16.h>
#include <stdint.h>

#define STAMPS 0

typedef short bf16x8 __attribute__((ext_vector_type(8)));
typedef short bf16x4 __attribute__((ext_vector_type(4)));
typedef float f32x4 __attribute__((ext_vector_type(4)));
typedef float f32x2 __attribute__((ext_vector_type(2)));
typedef __bf16 bf16v2 __attribute__((ext_vector_type(2)));
typedef unsigned u32x4 __attribute__((ext_vector_type(4)));
typedef unsigned u32x2 __attribute__((ext_vector_type(2)));
typedef unsigned long long u64;
#define LAS __attribute__((address_space(3)))
#define GAS __attribute__((address_space(1)))
typedef LAS const char* lcp;
typedef GAS u64 gu64;

constexpr int LDS_BYTES = 163840;
constexpr int A_SLOT = 40960, A_HALO = 122880, A_HROW = 1088;
constexpr int B_SLOT = 32768;

constexpr unsigned WS_EPOCH = 0;
constexpr unsigned WS_FO = 8192;
constexpr unsigned WS_FKV = 8192 + 65536;
constexpr unsigned FLAG_STRIDE = 128;
constexpr unsigned WS_KV = 262144;
constexpr unsigned WS_Z = WS_KV + 524288;
constexpr unsigned WS_O = 1048576;
constexpr unsigned WS_END = WS_O + 4194304;
constexpr unsigned WS_STAMP = 8388608;
constexpr unsigned SPIN_CAP = 1u << 18;

#define RLX_AGENT __ATOMIC_RELAXED, __HIP_MEMORY_SCOPE_AGENT
#define SBAR() __builtin_amdgcn_sched_barrier(0)
#define PIN(x) asm volatile("" : "+v"(x))
#define WAIT_BAR(N) asm volatile("s_waitcnt vmcnt(" #N ") lgkmcnt(0)\n\ts_barrier" ::: "memory")
#define DRAIN() asm volatile("s_waitcnt vmcnt(0)" ::: "memory")
#define MF32(a, b, c) __builtin_amdgcn_mfma_f32_16x16x32_bf16(a, b, c, 0, 0, 0)
#define MF16(a, b, c) __builtin_amdgcn_mfma_f32_16x16x16bf16_1k(a, b, c, 0, 0, 0)

#define STAMP(k) do { if ((k) == 0) { t0_ = __builtin_amdgcn_s_memrealtime(); tp_ = t0_; } else if ((k) == PROBE) tp_ = __builtin_amdgcn_s_memrealtime(); } while (0)

__device__ __forceinline__ void glds16(unsigned voff, const void* sbase, unsigned lds_addr) {
    unsigned keep;
    asm volatile("s_mov_b32 %0, m0\n\ts_mov_b32 m0, %3\n\ts_nop 0\n\tglobal_load_lds_dwordx4 %1, %2\n\ts_mov_b32 m0, %0"
                 : "=&s"(keep) : "v"(voff), "s"(sbase), "s"(lds_addr) : "memory");
}
__device__ __forceinline__ bf16x8 lds128(lcp p) { return *(const LAS bf16x8*)p; }
__device__ __forceinline__ bf16x4 ldstr(lcp p) { return __builtin_amdgcn_ds_read_tr16_b64_v4i16((LAS bf16x4*)p); }
__device__ __forceinline__ bf16x8 ldsfragT(lcp p) {
    bf16x4 a = ldstr(p), b = ldstr(p + 512);
    return __builtin_shufflevector(a, b, 0, 1, 2, 3, 4, 5, 6, 7);
}
__device__ __forceinline__ unsigned pk2(float a, float b) {
    f32x2 v = {a, b};
    return __builtin_bit_cast(unsigned, __builtin_convertvector(v, bf16v2));
}
__device__ __forceinline__ u32x2 pk4(f32x4 v) { return u32x2{pk2(v[0], v[1]), pk2(v[2], v[3])}; }
__device__ __forceinline__ bf16x4 as4(u32x2 v) { return __builtin_bit_cast(bf16x4, v); }
__device__ __forceinline__ bf16x8 as8(u32x2 a, u32x2 b) { return __builtin_bit_cast(bf16x8, u32x4{a[0], a[1], b[0], b[1]}); }
__device__ __forceinline__ u32x4 swap16(u32x2 a, u32x2 b) {
    auto x = __builtin_amdgcn_permlane16_swap(a[0], b[0], false, false);
    auto y = __builtin_amdgcn_permlane16_swap(a[1], b[1], false, false);
    return u32x4{x[0], y[0], x[1], y[1]};
}
__device__ __forceinline__ float xmax(float m) {
    auto a = __builtin_amdgcn_permlane16_swap(__float_as_uint(m), __float_as_uint(m), false, false);
    m = __builtin_fmaxf(__uint_as_float(a[0]), __uint_as_float(a[1]));
    auto b = __builtin_amdgcn_permlane32_swap(__float_as_uint(m), __float_as_uint(m), false, false);
    return __builtin_fmaxf(__uint_as_float(b[0]), __uint_as_float(b[1]));
}
__device__ __forceinline__ float phi(float t) {
    float e = __builtin_amdgcn_exp2f(t * 1.4426950408889634f);
    return (t > 0.f ? t + 1.f : e) + 1e-6f;
}
__device__ __forceinline__ f32x4 phi4(f32x4 v) { return f32x4{phi(v[0]), phi(v[1]), phi(v[2]), phi(v[3])}; }
template <int CTRL> __device__ __forceinline__ float dpp_add(float v) {
    return v + __int_as_float(__builtin_amdgcn_update_dpp(0, __float_as_int(v), CTRL, 0xf, 0xf, false));
}
__device__ __forceinline__ float row_sum(float v) {
    v = dpp_add<0x128>(v); v = dpp_add<0x124>(v); v = dpp_add<0x122>(v); v = dpp_add<0x121>(v);
    return v;
}

__device__ __forceinline__ void poll_flags(const char* ws, unsigned off, int n, u64 tag, int lane) {
    gu64* p = (gu64*)(ws + off + (unsigned)(lane < n ? lane : 0) * FLAG_STRIDE);
    for (unsigned spins = 0; spins < SPIN_CAP; ++spins) {
        u64 v = __hip_atomic_load(p, RLX_AGENT);
        if (__all(v == tag)) break;
        __builtin_amdgcn_s_sleep(1);
    }
    __builtin_amdgcn_fence(__ATOMIC_ACQUIRE, "wavefront");
}

__global__ void __launch_bounds__(512, 2)
fused_kernel(const char* __restrict__ xA, const char* __restrict__ wA, const char* __restrict__ woA,
             const char* __restrict__ xB, const char* __restrict__ wB, const char* __restrict__ woB,
             char* __restrict__ out, char* ws) {
    __shared__ __attribute__((aligned(16))) char smem[LDS_BYTES];
    const int tid = threadIdx.x, lane = tid & 63;
    const int w = __builtin_amdgcn_readfirstlane(tid >> 6);
    const int b = blockIdx.x, xcd = b & 7, idx = b >> 3;
    const int br = xcd >> 2, r = (xcd & 3) * 4 + (idx >> 3), g = idx & 7;
    const int s0 = r * 128;
    const int i16 = lane & 15, G = lane >> 4, u = w & 3, hf = w >> 2;
    const unsigned lds0 = (unsigned)(uintptr_t)smem;
    const lcp L = (lcp)smem;
    u64 t0_, tp_;
    STAMP(0);

    const char* x = br ? xB : xA;
    const char* wq = br ? wB : wA;
    const char* wo = br ? woB : woA;

    unsigned xoff[2];
#pragma unroll
    for (int e = 0; e < 2; ++e) {
        const int tok = s0 + (w >> 2) + 32 * (w & 3) + 2 * (8 * e + (lane >> 3));
        xoff[e] = (unsigned)tok * 1024u + (unsigned)(((lane & 7) ^ (4 * e + G)) * 16);
    }
    const int pc = lane & 7;
    const unsigned woff = (unsigned)(8 * w + (lane >> 3)) * 3072u + (unsigned)(g * 128) +
                          (unsigned)((((pc >> 1) ^ ((G & 1) | ((w & 1) << 1))) * 32) + (pc & 1) * 16);
    const unsigned crow = (unsigned)(32 * w + (lane >> 3));
    const unsigned cswz0 = (unsigned)(g * 128) + (unsigned)((((pc >> 1) ^ (G & 1)) * 32) + (pc & 1) * 16);
    const unsigned cswz1 = cswz0 ^ 64u;

    const int q4 = i16 >> 2, p4 = lane & 3;
    const int ftr = ((q4 >> 1) & 1) | ((G & 1) << 1);
    const unsigned trrow = (unsigned)((8 * G + q4) * 128 + p4 * 8);
    const unsigned xrd0 = (unsigned)(i16 * 128 + ((G ^ (i16 >> 1)) * 16));

    const __amdgpu_buffer_rsrc_t rs = __builtin_amdgcn_make_buffer_rsrc(ws, 0, WS_END, 0x00020000);
    const unsigned ep_off = WS_EPOCH + (unsigned)(br ? 16 : r) * 256u;
    u64 epoch;
    unsigned wo_lo, wo_hi;

    if (br == 0) {
#define A_STAGE(t) do { const unsigned d_ = lds0 + ((t) % 3) * A_SLOT; \
        glds16(xoff[0], x + (t) * 128, d_ + w * 2048); glds16(xoff[1], x + (t) * 128, d_ + w * 2048 + 1024); \
        glds16(woff, wq + (t) * 196608, d_ + 16384 + w * 1024); \
        glds16(woff, wq + (t) * 196608 + 1024, d_ + 24576 + w * 1024); \
        glds16(woff, wq + (t) * 196608 + 2048, d_ + 32768 + w * 1024); } while (0)
        A_STAGE(0);
#pragma unroll
        for (int e = 0; e < 2; ++e) {
            const int eta = 2 * w + e;
            int tok = s0 - 16 + (eta >> 3) + 2 * (eta & 7);
            tok = tok < 0 ? 0 : tok;
            const unsigned hoff = (unsigned)tok * 1024u + (unsigned)((lane ^ ((eta >> 3) << 1)) * 16);
            glds16(hoff, x, lds0 + A_HALO + eta * A_HROW);
        }
        A_STAGE(1);
        A_STAGE(2);
        asm volatile("s_load_dwordx2 %0, %1, %2\n\ts_waitcnt lgkmcnt(0)" : "=&s"(epoch) : "s"(ws), "s"(ep_off) : "memory");

        const int eta_r = i16 ^ (hf ? 0 : 8);
        const lcp hrd = L + A_HALO + eta_r * A_HROW + ((G ^ ((eta_r >> 3) << 1)) * 16);
        const lcp xr0 = L + xrd0 + hf * 8192, xr1 = L + (xrd0 ^ 64u) + hf * 8192;
        const lcp wr = L + 16384 + trrow + ((u ^ ftr) * 32);

        f32x4 aq[4], ak[5], av[5];
#pragma unroll
        for (int j = 0; j < 4; ++j) aq[j] = f32x4{0.f, 0.f, 0.f, 0.f};
#pragma unroll
        for (int j = 0; j < 5; ++j) { ak[j] = f32x4{0.f, 0.f, 0.f, 0.f}; av[j] = f32x4{0.f, 0.f, 0.f, 0.f}; }
        bf16x8 xf[2][5], wf[2][3];
#define A_READ(kk) do { const int so_ = (((kk) >> 1) % 3) * A_SLOT; const lcp xb_ = ((kk) & 1) ? xr1 : xr0; \
        _Pragma("unroll") for (int s_ = 0; s_ < 3; ++s_) wf[(kk) & 1][s_] = ldsfragT(wr + so_ + s_ * 8192 + ((kk) & 1) * 4096); \
        _Pragma("unroll") for (int j_ = 0; j_ < 4; ++j_) xf[(kk) & 1][j_ + 1] = lds128(xb_ + so_ + j_ * 2048); \
        xf[(kk) & 1][0] = lds128(hrd + (kk) * 64); } while (0)
#define A_MMA(kk) do { \
        _Pragma("unroll") for (int j_ = 0; j_ < 4; ++j_) { \
            aq[j_] = MF32(wf[(kk) & 1][0], xf[(kk) & 1][j_ + 1], aq[j_]); \
            ak[j_ + 1] = MF32(wf[(kk) & 1][1], xf[(kk) & 1][j_ + 1], ak[j_ + 1]); \
            av[j_ + 1] = MF32(xf[(kk) & 1][j_ + 1], wf[(kk) & 1][2], av[j_ + 1]); } \
        ak[0] = MF32(wf[(kk) & 1][1], xf[(kk) & 1][0], ak[0]); \
        av[0] = MF32(xf[(kk) & 1][0], wf[(kk) & 1][2], av[0]); } while (0)
#define CHUNK(src, pitch, dst) do { _Pragma("unroll") for (int n_ = 0; n_ < 4; ++n_) \
        glds16((crow + 8 * n_) * (pitch) + ((n_ & 1) ? cswz1 : cswz0), (src), lds0 + (dst) + w * 4096 + n_ * 1024); } while (0)

        WAIT_BAR(10);
        STAMP(1);
        A_READ(0);
#pragma unroll
        for (int t = 0; t < 8; ++t) {
            A_READ(2 * t + 1);
            SBAR();
            A_MMA(2 * t);
            SBAR();
            if (t < 7) {
                if (t < 6) WAIT_BAR(5); else WAIT_BAR(4);
                if (t < 5) A_STAGE(t + 3);
                else if (t == 5) CHUNK(wo + 262144, 1024u, 2 * A_SLOT);
                else CHUNK(wo, 1024u, 0);
                A_READ(2 * t + 2);
                SBAR();
            }
            A_MMA(2 * t + 1);
            SBAR();
        }
        wo_lo = 0; wo_hi = 2 * A_SLOT;
        STAMP(2);

        f32x4 ccur, cprev, cnone;
        unsigned mcur[2];
#pragma unroll
        for (int e = 0; e < 4; ++e) {
            const int key = 4 * G + e;
            ccur[e] = (key <= i16 && i16 - key <= 7) ? 0.f : -1e30f;
            cprev[e] = (key >= i16 + 9) ? 0.f : -1e30f;
            cnone[e] = (r == 0) ? -1e30f : cprev[e];
        }
#pragma unroll
        for (int e = 0; e < 2; ++e)
            mcur[e] = ((4 * G + 2 * e <= i16) ? 0xffffu : 0u) | ((4 * G + 2 * e + 1 <= i16) ? 0xffff0000u : 0u);
        u32x2 kb[5], vb[5], ob[4];
#pragma unroll
        for (int j = 0; j < 5; ++j) { kb[j] = pk4(ak[j]); vb[j] = pk4(av[j]); }
        const bf16x4 ones = {0x3f80, 0x3f80, 0x3f80, 0x3f80};
        const float cs = 0.25f * 1.4426950408889634f;
#pragma unroll
        for (int j = 0; j < 4; ++j) {
            const bf16x4 qb = as4(pk4(aq[j]));
            const f32x4 sc = MF16(as4(kb[j + 1]), qb, ccur);
            const f32x4 sp = MF16(as4(kb[j]), qb, j == 0 ? cnone : cprev);
            f32x4 s;
#pragma unroll
            for (int e = 0; e < 4; ++e) s[e] = __builtin_fmaxf(sc[e], sp[e]);
            const float m = xmax(__builtin_fmaxf(__builtin_fmaxf(s[0], s[1]), __builtin_fmaxf(s[2], s[3])));
            const float nm = -m * cs;
            f32x4 p;
#pragma unroll
            for (int e = 0; e < 4; ++e) p[e] = __builtin_amdgcn_exp2f(__builtin_fmaf(s[e], cs, nm));
            const u32x2 pb = pk4(p);
            const u32x2 pcur = {pb[0] & mcur[0], pb[1] & mcur[1]}, pprev = {pb[0] & ~mcur[0], pb[1] & ~mcur[1]};
            const f32x4 sum = MF16(ones, as4(pb), (f32x4{0.f, 0.f, 0.f, 0.f}));
            const f32x4 o = MF32(as8(vb[j], vb[j + 1]), as8(pprev, pcur), (f32x4{0.f, 0.f, 0.f, 0.f}));
            const float inv = __builtin_amdgcn_rcpf(sum[0]);
            ob[j] = pk4(f32x4{o[0] * inv, o[1] * inv, o[2] * inv, o[3] * inv});
        }
        const int h = 4 * g + u;
#pragma unroll
        for (int jp = 0; jp < 2; ++jp) {
            const u32x4 v = swap16(ob[2 * jp], ob[2 * jp + 1]);
            const int T = 4 * hf + 2 * jp + (G & 1);
            const unsigned off = WS_O + (unsigned)((((r * 8 + T) * 16 + (h >> 1)) * 4 + 2 * (h & 1) + (G >> 1)) * 256 + i16 * 16);
            __builtin_amdgcn_raw_buffer_store_b128(v, rs, off, 0, 16);
        }
    } else {
#define B_STAGE(t) do { const unsigned d_ = lds0 + ((t) % 5) * B_SLOT; \
        glds16(xoff[0], x + (t) * 128, d_ + w * 2048); glds16(xoff[1], x + (t) * 128, d_ + w * 2048 + 1024); \
        glds16(woff, wq + (t) * 196608 + 1024, d_ + 16384 + w * 1024); \
        glds16(woff, wq + (t) * 196608 + 2048, d_ + 24576 + w * 1024); } while (0)
        B_STAGE(0); B_STAGE(1); B_STAGE(2); B_STAGE(3); B_STAGE(4);
        asm volatile("s_load_dwordx2 %0, %1, %2\n\ts_waitcnt lgkmcnt(0)" : "=&s"(epoch) : "s"(ws), "s"(ep_off) : "memory");

        unsigned xa[4];
#pragma unroll
        for (int j = 0; j < 4; ++j) xa[j] = xrd0 + (unsigned)((4 * hf + (j ^ u)) * 2048);
        const lcp wr = L + 16384 + trrow + ((u ^ ftr) * 32);
        f32x4 ak[4], av[4];
#pragma unroll
        for (int j = 0; j < 4; ++j) { ak[j] = f32x4{0.f, 0.f, 0.f, 0.f}; av[j] = f32x4{0.f, 0.f, 0.f, 0.f}; }
        bf16x8 xs[16], xf[2][3], wf[2][2];
#define B_READ(kk) do { const int so_ = (((kk) >> 1) % 5) * B_SLOT; const unsigned xx_ = ((kk) & 1) ? 64u : 0u; \
        _Pragma("unroll") for (int s_ = 0; s_ < 2; ++s_) wf[(kk) & 1][s_] = ldsfragT(wr + so_ + s_ * 8192 + ((kk) & 1) * 4096); \
        xs[kk] = lds128(L + (xa[0] ^ xx_) + so_); \
        _Pragma("unroll") for (int j_ = 1; j_ < 4; ++j_) xf[(kk) & 1][j_ - 1] = lds128(L + (xa[j_] ^ xx_) + so_); } while (0)
#define B_MMA(kk) do { \
        ak[0] = MF32(xs[kk], wf[(kk) & 1][0], ak[0]); av[0] = MF32(xs[kk], wf[(kk) & 1][1], av[0]); \
        _Pragma("unroll") for (int j_ = 1; j_ < 4; ++j_) { \
            ak[j_] = MF32(xf[(kk) & 1][j_ - 1], wf[(kk) & 1][0], ak[j_]); \
            av[j_] = MF32(xf[(kk) & 1][j_ - 1], wf[(kk) & 1][1], av[j_]); } } while (0)

        WAIT_BAR(16);
        STAMP(1);
        B_READ(0);
#pragma unroll
        for (int t = 0; t < 8; ++t) {
            B_READ(2 * t + 1);
            SBAR();
            B_MMA(2 * t);
            SBAR();
            if (t < 7) {
                WAIT_BAR(12);
                if (t < 3) B_STAGE(t + 5);
                else if (t == 3) CHUNK(wq, 3072u, 3 * B_SLOT);
                else if (t == 4) CHUNK(wq + 786432, 3072u, 4 * B_SLOT);
                else if (t == 5) CHUNK(wo, 1024u, 0);
                B_READ(2 * t + 2);
                SBAR();
            }
            B_MMA(2 * t + 1);
            SBAR();
        }
        STAMP(2);
        wo_lo = 0; wo_hi = 2 * B_SLOT;

        const bf16x8 ones8 = {0x3f80, 0x3f80, 0x3f80, 0x3f80, 0x3f80, 0x3f80, 0x3f80, 0x3f80};
        f32x4 kv = {0.f, 0.f, 0.f, 0.f}, zz = {0.f, 0.f, 0.f, 0.f};
#pragma unroll
        for (int jp = 0; jp < 2; ++jp) {
            const bf16x8 pkf = as8(pk4(phi4(ak[2 * jp])), pk4(phi4(ak[2 * jp + 1])));
            const bf16x8 vf = as8(pk4(av[2 * jp]), pk4(av[2 * jp + 1]));
            kv = MF32(pkf, vf, kv);
            zz = MF32(pkf, ones8, zz);
        }
        LAS char* scr = (LAS char*)smem + B_SLOT;
        if (hf) {
            *(LAS f32x4*)(scr + u * 2048 + lane * 16) = kv;
            *(LAS f32x4*)(scr + u * 2048 + 1024 + lane * 16) = zz;
        }
        if (tid == 0) *(LAS unsigned*)(scr + 8192) = 0u;
        WAIT_BAR(4);
        const int h = 4 * g + u;
        const u64 tag = epoch + 1;
        if (!hf) {
            kv += *(const LAS f32x4*)(scr + u * 2048 + lane * 16);
            zz += *(const LAS f32x4*)(scr + u * 2048 + 1024 + lane * 16);
            __builtin_amdgcn_raw_buffer_store_b128(__builtin_bit_cast(u32x4, kv), rs, WS_KV + (unsigned)((h * 16 + r) * 1024 + lane * 16), 0, 16);
            if (i16 == 0)
                __builtin_amdgcn_raw_buffer_store_b128(__builtin_bit_cast(u32x4, zz), rs, WS_Z + (unsigned)((h * 16 + r) * 128 + G * 16), 0, 16);
            DRAIN();
            if (lane == 0) {
                const unsigned n = __hip_atomic_fetch_add((LAS unsigned*)(scr + 8192), 1u, __ATOMIC_RELAXED, __HIP_MEMORY_SCOPE_WORKGROUP);
                if (n == 3) __hip_atomic_store((gu64*)(ws + WS_FKV + (unsigned)(g * 16 + r) * FLAG_STRIDE), tag, RLX_AGENT);
            }
        }
        STAMP(3);
        CHUNK(wo + 262144, 1024u, 2 * B_SLOT);

        f32x4 aq[4];
#pragma unroll
        for (int c = 0; c < 4; ++c) aq[c] = f32x4{0.f, 0.f, 0.f, 0.f};
#pragma unroll
        for (int kk = 0; kk < 16; ++kk) {
#pragma unroll
            for (int c = 0; c < 4; ++c) {
                const bf16x8 f = ldsfragT(L + 3 * B_SLOT + trrow + ((c ^ ftr) * 32) + kk * 4096);
                aq[c] = MF32(f, xs[kk], aq[c]);
            }
        }
        u32x2 pq[4];
#pragma unroll
        for (int c = 0; c < 4; ++c) pq[c] = pk4(phi4(aq[c]));
        STAMP(4);

        if (w == 0) poll_flags(ws, WS_FKV + (unsigned)(g * 16) * FLAG_STRIDE, 16, tag, lane);
        asm volatile("s_barrier" ::: "memory");
        STAMP(5);
        u32x4 pv[8];
#pragma unroll
        for (int j = 0; j < 8; ++j)
            pv[j] = __builtin_amdgcn_raw_buffer_load_b128(rs, WS_KV + (unsigned)((h * 16 + 8 * hf + j) * 1024 + lane * 16), 0, 16);
        const u32x4 zv = __builtin_amdgcn_raw_buffer_load_b128(rs, WS_Z + (unsigned)((h * 16 + i16) * 128 + G * 16), 0, 16);
        f32x4 ks = __builtin_bit_cast(f32x4, pv[0]);
#pragma unroll
        for (int j = 1; j < 8; ++j) ks += __builtin_bit_cast(f32x4, pv[j]);
        LAS char* scr2 = scr + 16384;
        *(LAS f32x4*)(scr2 + w * 1024 + lane * 16) = ks;
        if (!hf) {
            const f32x4 zf = __builtin_bit_cast(f32x4, zv);
            const f32x4 zs = {row_sum(zf[0]), row_sum(zf[1]), row_sum(zf[2]), row_sum(zf[3])};
            *(LAS u32x2*)(scr2 + 8192 + u * 512 + lane * 8) = pk4(zs);
        }
        asm volatile("s_waitcnt lgkmcnt(0)\n\ts_barrier" ::: "memory");
        u32x2 ob[4];
#pragma unroll
        for (int c = 0; c < 4; ++c) {
            const f32x4 k0 = *(const LAS f32x4*)(scr2 + c * 1024 + lane * 16);
            const f32x4 k1 = *(const LAS f32x4*)(scr2 + 4096 + c * 1024 + lane * 16);
            const u32x2 zb = *(const LAS u32x2*)(scr2 + 8192 + c * 512 + lane * 8);
            const f32x4 num = MF16(as4(pk4(k0 + k1)), as4(pq[c]), (f32x4{0.f, 0.f, 0.f, 0.f}));
            const f32x4 den = MF16(as4(zb), as4(pq[c]), (f32x4{1e-6f, 1e-6f, 1e-6f, 1e-6f}));
            const float inv = __builtin_amdgcn_rcpf(den[0]);
            ob[c] = pk4(f32x4{num[0] * inv, num[1] * inv, num[2] * inv, num[3] * inv});
        }
#pragma unroll
        for (int cp = 0; cp < 2; ++cp) {
            const u32x4 v = swap16(ob[2 * cp], ob[2 * cp + 1]);
            const unsigned off = WS_O + (unsigned)(((((16 + r) * 8 + w) * 16 + 2 * g + cp) * 4 + 2 * (G & 1) + (G >> 1)) * 256 + i16 * 16);
            __builtin_amdgcn_raw_buffer_store_b128(v, rs, off, 0, 16);
        }
    }

    const u64 tag = epoch + 1;
    const unsigned fo = WS_FO + (unsigned)((br * 16 + r) * 8) * FLAG_STRIDE;
    STAMP(6);
    WAIT_BAR(0);
    if (w == 0) {
        if (lane == 0) __hip_atomic_store((gu64*)(ws + fo + (unsigned)g * FLAG_STRIDE), tag, RLX_AGENT);
        STAMP(7);
        poll_flags(ws, fo, 8, tag, lane);
        if (lane == 0 && g == 0 && (br == 0 || r == 0)) __hip_atomic_store((gu64*)(ws + ep_off), tag, RLX_AGENT);
    }
    asm volatile("s_barrier" ::: "memory");
    STAMP(8);
    u32x4 of[16];
#pragma unroll
    for (int s = 0; s < 16; ++s)
        of[s] = __builtin_amdgcn_raw_buffer_load_b128(rs, WS_O + (unsigned)((((br * 16 + r) * 8 + w) * 16 + s) * 1024 + lane * 16), 0, 16);
    SBAR();
    bf16x8 wpre[8][4];
#pragma unroll
    for (int s = 0; s < 8; ++s)
#pragma unroll
        for (int c = 0; c < 4; ++c) wpre[s][c] = ldsfragT(L + wo_lo + trrow + ((c ^ ftr) * 32) + s * 4096);
    SBAR();
    f32x4 acc[4];
#pragma unroll
    for (int c = 0; c < 4; ++c) acc[c] = f32x4{0.f, 0.f, 0.f, 0.f};
#pragma unroll
    for (int s = 0; s < 8; ++s)
#pragma unroll
        for (int c = 0; c < 4; ++c) {
            acc[c] = MF32(wpre[s][c], __builtin_bit_cast(bf16x8, of[s]), acc[c]);
            wpre[s][c] = ldsfragT(L + wo_hi + trrow + ((c ^ ftr) * 32) + s * 4096);
        }
#pragma unroll
    for (int s = 0; s < 8; ++s)
#pragma unroll
        for (int c = 0; c < 4; ++c) acc[c] = MF32(wpre[s][c], __builtin_bit_cast(bf16x8, of[8 + s]), acc[c]);
    STAMP(9);
    const int tok = s0 + (w >> 2) + 32 * (w & 3) + 2 * i16;
    char* orow = out + (size_t)tok * 2048 + br * 1024 + g * 128 + (G & 1) * 32 + (G >> 1) * 16;
#pragma unroll
    for (int cp = 0; cp < 2; ++cp) {
        const u32x4 v = swap16(pk4(acc[2 * cp]), pk4(acc[2 * cp + 1]));
        asm volatile("global_store_dwordx4 %0, %1, off sc1" :: "v"(orow + cp * 64), "v"(v) : "memory");
    }
    STAMP(10);
    if (w == 0 && b == 0) {
        const u64 tend = t0_ + 5000 + 8 * (tp_ - t0_);
        while (__builtin_amdgcn_s_memrealtime() < tend) __builtin_amdgcn_s_sleep(1);
    }
}

extern "C" void kernel_launch(void* const* d_in, const int* in_sizes, int n_in,
                              void* d_out, int out_size, void* d_ws, size_t ws_size,
                              hipStream_t stream) {
    fused_kernel<<<256, 512, 0, stream>>>((const char*)d_in[0], (const char*)d_in[1], (const char*)d_in[2],
                                                  (const char*)d_in[3], (const char*)d_in[4], (const char*)d_in[5],
                                                  (char*)d_out, (char*)d_ws);
}
```

```cpp
#include <hip/hip_runtime.h>
#include <hip/hip_bf16.h>
#include <stdint.h>

#define STAMPS 0
#define P2_DELAY 10
#define PSLEEP 2
#define LPRIO 3
#define GSLEEP 2
#define OUT_SC1 1

typedef short bf16x8 __attribute__((ext_vector_type(8)));
typedef short bf16x4 __attribute__((ext_vector_type(4)));
typedef float f32x4 __attribute__((ext_vector_type(4)));
typedef float f32x2 __attribute__((ext_vector_type(2)));
typedef __bf16 bf16v2 __attribute__((ext_vector_type(2)));
typedef unsigned u32x4 __attribute__((ext_vector_type(4)));
typedef unsigned u32x2 __attribute__((ext_vector_type(2)));
typedef unsigned long long u64;
#define LAS __attribute__((address_space(3)))
#define GAS __attribute__((address_space(1)))
typedef LAS const char* lcp;
typedef GAS u64 gu64;

constexpr int LDS_BYTES = 163840;
constexpr int A_XS = 18432, A_XH = 9216, A_W = 3 * A_XS, A_WS = 24576, A_WOLO = A_W + 3 * A_WS, A_WOHI = 2 * A_XS;
constexpr int B_XS = 16384, B_W = 3 * B_XS, B_WS = 16384, B_WQ = 98304, B_WOLO = 32768, B_WOHI = 65536, B_SCR = 0;

constexpr unsigned WS_EPOCH = 0;
constexpr unsigned WS_FLAG = 8192;
constexpr unsigned FLAG_STRIDE = 128;
constexpr unsigned WS_KV = 65536;
constexpr unsigned WS_Z = WS_KV + 262144;
constexpr unsigned WS_O = 1048576;
constexpr unsigned WS_END = WS_O + 4194304;
constexpr unsigned WS_STAMP = 8388608;
constexpr unsigned SPIN_CAP = 1u << 16;
constexpr unsigned EMPTY = 0xffffffffu;

#define RLX_AGENT __ATOMIC_RELAXED, __HIP_MEMORY_SCOPE_AGENT
#define SBAR() __builtin_amdgcn_sched_barrier(0)
#define BAR() asm volatile("s_barrier" ::: "memory")
#define LBAR() asm volatile("s_waitcnt lgkmcnt(0)\n\ts_barrier" ::: "memory")
#define VBAR(N) asm volatile("s_waitcnt vmcnt(" #N ")\n\ts_barrier" ::: "memory")
#define VLBAR(N) asm volatile("s_waitcnt vmcnt(" #N ") lgkmcnt(0)\n\ts_barrier" ::: "memory")
#define SVBAR(N, k) do { if ((k) < 8) TL(2 * (k) + 1); VBAR(N); if ((k) < 8) TL(2 * (k) + 2); else TL(17); } while (0)
#define SGB(mask, n) __builtin_amdgcn_sched_group_barrier(mask, n, 0)
#define WEAVE(n) do { _Pragma("unroll") for (int i_ = 0; i_ < (n); ++i_) { SGB(0x008, 1); SGB(0x100, 2); } } while (0)
#define MF32(a, b, c) __builtin_amdgcn_mfma_f32_16x16x32_bf16(a, b, c, 0, 0, 0)
#define MF16(a, b, c) __builtin_amdgcn_mfma_f32_16x16x16bf16_1k(a, b, c, 0, 0, 0)
#define LD128(off) __builtin_amdgcn_raw_buffer_load_b128(rs, off, 0, 16)
#define ST128(v, off) __builtin_amdgcn_raw_buffer_store_b128(v, rs, off, 0, 16)
#define ST64(v, off) __builtin_amdgcn_raw_buffer_store_b64(v, rs, off, 0, 16)

#define CSTAMP(k) do {} while (0)
#define LSTAMP(k) do {} while (0)

#define TLINE 0
#define TL(k) do {} while (0)
#define TLARG
#define TLPASS
#define TLDUMP(role) do {} while (0)

__device__ __forceinline__ void glds16(unsigned voff, const void* sbase, unsigned lds_addr) {
    unsigned keep;
    asm volatile("s_mov_b32 %0, m0\n\ts_mov_b32 m0, %3\n\ts_nop 0\n\tglobal_load_lds_dwordx4 %1, %2\n\ts_mov_b32 m0, %0"
                 : "=&s"(keep) : "v"(voff), "s"(sbase), "s"(lds_addr) : "memory");
}
__device__ __forceinline__ bf16x8 lds128(lcp p) { return *(const LAS bf16x8*)p; }
__device__ __forceinline__ bf16x4 ldstr(lcp p) { return __builtin_amdgcn_ds_read_tr16_b64_v4i16((LAS bf16x4*)p); }
__device__ __forceinline__ bf16x8 ldsfragT(lcp p) {
    bf16x4 a = ldstr(p), b = ldstr(p + 512);
    return __builtin_shufflevector(a, b, 0, 1, 2, 3, 4, 5, 6, 7);
}
__device__ __forceinline__ unsigned pk2(float a, float b) {
    f32x2 v = {a, b};
    return __builtin_bit_cast(unsigned, __builtin_convertvector(v, bf16v2));
}
__device__ __forceinline__ u32x2 pk4(f32x4 v) { return u32x2{pk2(v[0], v[1]), pk2(v[2], v[3])}; }
__device__ __forceinline__ bf16x4 as4(u32x2 v) { return __builtin_bit_cast(bf16x4, v); }
__device__ __forceinline__ bf16x8 as8(u32x2 a, u32x2 b) { return __builtin_bit_cast(bf16x8, u32x4{a[0], a[1], b[0], b[1]}); }
__device__ __forceinline__ u32x4 swap16(u32x2 a, u32x2 b) {
    auto x = __builtin_amdgcn_permlane16_swap(a[0], b[0], false, false);
    auto y = __builtin_amdgcn_permlane16_swap(a[1], b[1], false, false);
    return u32x4{x[0], y[0], x[1], y[1]};
}
__device__ __forceinline__ float fmx(float a, float b) { return __builtin_elementwise_maximum(a, b); }
__device__ __forceinline__ float xmax(float m) {
    auto a = __builtin_amdgcn_permlane16_swap(__float_as_uint(m), __float_as_uint(m), false, false);
    m = fmx(__uint_as_float(a[0]), __uint_as_float(a[1]));
    auto b = __builtin_amdgcn_permlane32_swap(__float_as_uint(m), __float_as_uint(m), false, false);
    return fmx(__uint_as_float(b[0]), __uint_as_float(b[1]));
}
__device__ __forceinline__ float phi(float te) {
    const float e = __builtin_amdgcn_exp2f(__builtin_fmaf(te, 1.4426950408889634f, -1.4426950408889634e-6f));
    return __builtin_amdgcn_fmed3f(e, 0.f, 1.f) + fmx(te, 1e-6f);
}
__device__ __forceinline__ f32x4 phi4(f32x4 v) { return f32x4{phi(v[0]), phi(v[1]), phi(v[2]), phi(v[3])}; }
template <int CTRL> __device__ __forceinline__ float dpp_add(float v) {
    return v + __int_as_float(__builtin_amdgcn_update_dpp(0, __float_as_int(v), CTRL, 0xf, 0xf, false));
}
__device__ __forceinline__ float row_sum(float v) {
    v = dpp_add<0x128>(v); v = dpp_add<0x124>(v); v = dpp_add<0x122>(v); v = dpp_add<0x121>(v);
    return v;
}
__device__ __forceinline__ unsigned fill1(unsigned v) { return v < EMPTY - 1u ? v : EMPTY - 1u; }
__device__ __forceinline__ u32x4 fill4(u32x4 v) { return u32x4{fill1(v[0]), fill1(v[1]), fill1(v[2]), fill1(v[3])}; }
__device__ __forceinline__ unsigned umax(unsigned a, unsigned b) { return a > b ? a : b; }
__device__ __forceinline__ bool missing(u32x4 v) {
    return __builtin_amdgcn_ballot_w64(umax(umax(v[0], v[1]), umax(v[2], v[3])) == EMPTY) != 0;
}
__device__ __forceinline__ bool missing2(u32x4 a, u32x4 b) {
    const unsigned m = umax(umax(umax(umax(a[0], a[1]), a[2]), umax(umax(a[3], b[0]), b[1])), umax(b[2], b[3]));
    return __builtin_amdgcn_ballot_w64(m == EMPTY) != 0;
}
__device__ __forceinline__ int fresh_lane() {
    int l;
    asm volatile("v_mbcnt_lo_u32_b32 %0, -1, 0\n\tv_mbcnt_hi_u32_b32 %0, -1, %0" : "=v"(l));
    return l;
}
__device__ __forceinline__ unsigned xread(int lane) {
    const int i16 = lane & 15, G = lane >> 4;
    return (unsigned)(i16 * 128 + ((G ^ (i16 >> 1)) * 16));
}
__device__ __forceinline__ unsigned trread(int lane, int c) {
    const int q4 = (lane & 15) >> 2, G = lane >> 4;
    const int ftr = ((q4 >> 1) & 1) | ((G & 1) << 1);
    return (unsigned)((8 * G + q4) * 128 + (lane & 3) * 8 + ((c ^ ftr) * 32));
}
__device__ __forceinline__ float bflo(unsigned v) { return __uint_as_float(v << 16); }
__device__ __forceinline__ float bfhi(unsigned v) { return __uint_as_float(v & 0xffff0000u); }

__device__ __forceinline__ void phase2(lcp L, unsigned wo_lo, unsigned wo_hi, __amdgpu_buffer_rsrc_t rs, unsigned orow,
                                       int w, int g, char* out, unsigned obase, char* ws TLARG) {
    const int lane = fresh_lane(), i16 = lane & 15, G = lane >> 4;
    const unsigned ol = orow + (unsigned)(w * 16384 + lane * 16);
    u32x4 of[16];
#define OLD(j, e) __builtin_amdgcn_raw_buffer_load_b128(rs, ol + (e) * 1024, ((g + 1 + (j)) & 7) * 2048, 16)
    TL(20);
    if (P2_DELAY == 0) VBAR(0);
    else { BAR(); __builtin_amdgcn_s_sleep(P2_DELAY); }
    TL(27);
    of[0] = OLD(0, 0); of[1] = OLD(0, 1);
    lcp wl[4], wh[4];
#pragma unroll
    for (int c = 0; c < 4; ++c) { wl[c] = L + wo_lo + trread(lane, c); wh[c] = L + wo_hi + trread(lane, c); }
    bf16x8 wf[2][8];
#define WRD(buf, j) do { _Pragma("unroll") for (int q_ = 0; q_ < 8; ++q_) \
        wf[buf][q_] = ldsfragT(((j) < 4 ? wl[q_ & 3] : wh[q_ & 3]) + ((j) & 3) * 8192 + (q_ >> 2) * 4096); } while (0)
#define OMM(buf, j) do { _Pragma("unroll") for (int q_ = 0; q_ < 8; ++q_) \
        acc[q_ & 3] = MF32(wf[buf][q_], __builtin_bit_cast(bf16x8, of[2 * (j) + (q_ >> 2)]), acc[q_ & 3]); } while (0)
    f32x4 acc[4];
#pragma unroll
    for (int c = 0; c < 4; ++c) acc[c] = f32x4{0.f, 0.f, 0.f, 0.f};
    WRD(0, 0);
    WRD(1, 1);
#pragma nounroll
    for (unsigned tries = 0; tries < SPIN_CAP && missing2(of[0], of[1]); ++tries) {
        __builtin_amdgcn_s_sleep(PSLEEP);
        asm volatile("" ::: "memory");
        of[0] = OLD(0, 0); of[1] = OLD(0, 1);
    }
    TL(21);
    CSTAMP(8);
#pragma unroll
    for (int j = 1; j < 8; ++j) { of[2 * j] = OLD(j, 0); of[2 * j + 1] = OLD(j, 1); }
    OMM(0, 0);
    unsigned pend = 0;
#pragma unroll
    for (int j = 1; j < 8; ++j) {
        if (j < 7) WRD((j + 1) & 1, j + 1);
        if (missing2(of[2 * j], of[2 * j + 1])) {
            asm volatile("" ::: "memory");
            of[2 * j] = OLD(j, 0); of[2 * j + 1] = OLD(j, 1);
            pend |= 1u << j;
        } else {
            OMM(j & 1, j);
        }
    }
#pragma nounroll
    for (unsigned tries = 0; pend && tries < SPIN_CAP; ++tries) {
#pragma unroll
        for (int j = 1; j < 8; ++j) {
            if (pend & (1u << j)) {
                WRD(0, j);
                if (missing2(of[2 * j], of[2 * j + 1])) {
                    asm volatile("" ::: "memory");
                    of[2 * j] = OLD(j, 0); of[2 * j + 1] = OLD(j, 1);
                } else {
                    OMM(0, j);
                    pend &= ~(1u << j);
                }
            }
        }
    }
    TL(22);
    CSTAMP(9);
    const __amdgpu_buffer_rsrc_t ro = __builtin_amdgcn_make_buffer_rsrc(out, 0, 4194304, 0x00020000);
    const unsigned op = obase + (unsigned)(i16 * 4096 + (G & 1) * 32 + (G >> 1) * 16);
#pragma unroll
    for (int cp = 0; cp < 2; ++cp) {
        const u32x4 v = swap16(pk4(acc[2 * cp]), pk4(acc[2 * cp + 1]));
        __builtin_amdgcn_raw_buffer_store_b128(v, ro, op + cp * 64, 0, OUT_SC1 ? 16 : 0);
    }
    CSTAMP(10);
}

__global__ void __launch_bounds__(768, 3)
fused_kernel(const char* __restrict__ xA, const char* __restrict__ wA, const char* __restrict__ woA,
             const char* __restrict__ xB, const char* __restrict__ wB, const char* __restrict__ woB,
             char* __restrict__ out, char* ws) {
    __shared__ __attribute__((aligned(16))) char smem[LDS_BYTES];
    const int w = __builtin_amdgcn_readfirstlane(threadIdx.x >> 6);
    const int b = blockIdx.x, xcd = b & 7, idx = b >> 3;
    const int br = xcd >> 2, r = (xcd & 3) * 4 + (idx >> 3), g = idx & 7;
    const int s0 = r * 128;
    const lcp L = (lcp)smem;
    const __amdgpu_buffer_rsrc_t rs = __builtin_amdgcn_make_buffer_rsrc(ws, 0, WS_END, 0x00020000);
    const unsigned ep_off = WS_EPOCH + (unsigned)(br ? 16 : r) * 256u;
    const unsigned orow = WS_O + (unsigned)((br * 16 + r) * 131072);
    u64 epoch;
    TL(0);
    asm volatile("" :: "s"(xA), "s"(wA), "s"(woA), "s"(xB), "s"(wB), "s"(woB), "s"(out), "s"(ws));
#define LOAD_EPOCH() asm volatile("s_load_dwordx2 %0, %1, %2" : "=&s"(epoch) : "s"(ws), "s"(ep_off) : "memory")

    if (w >= 8) {
        const int lane = fresh_lane(), i16 = lane & 15, G = lane >> 4;
        LSTAMP(11);
        const int lw = w - 8;
        const unsigned lds0 = (unsigned)(uintptr_t)smem;
        const char* x = br ? xB : xA;
        const char* wq = br ? wB : wA;
        const char* wo = br ? woB : woA;
        const int pc = lane & 7, rw = lane >> 3;
        unsigned xoff[2];
#pragma unroll
        for (int e = 0; e < 2; ++e)
            xoff[e] = (unsigned)(s0 + 32 * lw + 2 * (8 * e + rw)) * 1024u + (unsigned)((pc ^ (4 * e + G)) * 16);
        const unsigned usw = (unsigned)((((pc >> 1) ^ ((G & 1) | ((lw & 1) << 1))) * 32) + (pc & 1) * 16 + g * 128);
        const unsigned woff = (unsigned)(8 * lw + rw) * 3072u + usw;
        const unsigned ooff = (unsigned)(8 * lw + rw) * 1024u + usw;
#define XP(t, d, hs, p) glds16(xoff[(p) & 1], x + (t) * 128 + ((p) >> 1) * 1024, (d) + lw * 2048 + ((p) >> 1) * (hs) + ((p) & 1) * 1024)
#define WP(t, d, sg, p) glds16(woff, wq + (t) * 196608 + (sg) * 1024 + (p) * 98304, (d) + (lw + 4 * (p)) * 1024)
#define QP(n, d) glds16(woff, wq + (n) * 98304, (d) + (n) * 4096 + lw * 1024)
#define OP(n, d) glds16(ooff, wo + (((n) + 2 * g + 2) & 15) * 32768, (d) + ((n) & 7) * 4096 + lw * 1024)
        __builtin_amdgcn_s_setprio(LPRIO);
        gu64* const fl = (gu64*)(ws + WS_FLAG + (unsigned)(br * 128 + (br && lane < 16 ? lane * 8 + g : r * 8 + (lane & 7))) * FLAG_STRIDE);
        u64 fv;
#define EPOCH_IN() asm volatile("s_waitcnt lgkmcnt(0)" : "+s"(epoch) :: "memory")
#define CHECK_FLAGS() do { \
        _Pragma("nounroll") for (unsigned spins = 0; spins < SPIN_CAP && !__all(fv == epoch + 1); ++spins) { \
            __builtin_amdgcn_s_sleep(1); \
            fv = __hip_atomic_load(fl, RLX_AGENT); } \
        __builtin_amdgcn_fence(__ATOMIC_ACQUIRE, "wavefront"); } while (0)
        if (br == 0) {
            const int eta = 4 * (lw & 1) + (rw & 3);
            int ht = s0 - 16 + (lw >> 1) + 2 * eta;
            ht = ht < 0 ? 0 : ht;
            const unsigned hoff = (unsigned)ht * 1024u + (unsigned)((pc ^ (4 + (eta >> 1))) * 16);
            const unsigned hdst = lds0 + (unsigned)((lw >> 1) * A_XH + 8192 + (lw & 1) * 512);
#define A_LO(t) do { const unsigned dx_ = lds0 + ((t) % 3) * A_XS, dw_ = lds0 + A_W + ((t) % 3) * A_WS; \
        XP(t, dx_, A_XH, 0); WP(t, dw_, 0, 0); WP(t, dw_ + 8192, 1, 0); WP(t, dw_ + 16384, 2, 0); XP(t, dx_, A_XH, 1); XP(t, dx_, A_XH, 2); } while (0)
#define A_HI(t) do { const unsigned dx_ = lds0 + ((t) % 3) * A_XS, dw_ = lds0 + A_W + ((t) % 3) * A_WS; \
        XP(t, dx_, A_XH, 3); if (lane < 32) glds16(hoff, x + (t) * 128, hdst + ((t) % 3) * A_XS); \
        WP(t, dw_, 0, 1); WP(t, dw_ + 8192, 1, 1); WP(t, dw_ + 16384, 2, 1); } while (0)
            A_LO(0); A_HI(0); A_LO(1);
            LOAD_EPOCH();
            SVBAR(6, 0);
            A_HI(1); A_LO(2);
            SVBAR(6, 1);
            A_HI(2); A_LO(3); A_HI(3);
            SVBAR(11, 2);
            A_LO(4); A_HI(4);
            EPOCH_IN();
            SVBAR(11, 3);
            A_LO(5); A_HI(5);
            SVBAR(11, 4);
            A_LO(6); A_HI(6);
            SVBAR(11, 5);
            A_LO(7); A_HI(7);
            SVBAR(11, 6);
            fv = __hip_atomic_load(fl, RLX_AGENT);
#pragma unroll
            for (int n = 0; n < 8; ++n) OP(n, lds0 + A_WOLO);
            SVBAR(8, 7);
#pragma unroll
            for (int n = 8; n < 16; ++n) OP(n, lds0 + A_WOHI);
            TL(18);
            CHECK_FLAGS();
            TL(19);
            VBAR(0);
            TL(21);
            TLDUMP(1);
            if (lw == 0 && g == 0) __hip_atomic_store((gu64*)(ws + ep_off), epoch + 1, RLX_AGENT);
        } else {
#define B_ST(t) do { const unsigned dx_ = lds0 + ((t) % 3) * B_XS, dw_ = lds0 + B_W + ((t) % 3) * B_WS; \
        XP(t, dx_, 8192, 0); WP(t, dw_, 1, 0); WP(t, dw_ + 8192, 2, 0); XP(t, dx_, 8192, 1); XP(t, dx_, 8192, 2); XP(t, dx_, 8192, 3); \
        WP(t, dw_, 1, 1); WP(t, dw_ + 8192, 2, 1); } while (0)
            B_ST(0); B_ST(1);
            LOAD_EPOCH();
            SVBAR(8, 0); B_ST(2);
            SVBAR(8, 1); B_ST(3);
            SVBAR(8, 2); B_ST(4);
            EPOCH_IN();
            SVBAR(8, 3); B_ST(5);
            SVBAR(8, 4); B_ST(6);
            SVBAR(8, 5); B_ST(7);
            SVBAR(8, 6);
            fv = __hip_atomic_load(fl, RLX_AGENT);
#pragma unroll
            for (int n = 0; n < 8; ++n) QP(n, lds0 + B_WQ);
            SVBAR(8, 7);
#pragma unroll
            for (int n = 8; n < 16; ++n) QP(n, lds0 + B_WQ);
            CHECK_FLAGS();
            TL(19);
            VBAR(0);
            TL(17);
            __builtin_amdgcn_s_sleep(5);
#pragma unroll
            for (int n = 0; n < 8; ++n) OP(n, lds0 + B_WOLO);
            TL(18);
            const int h = 4 * g + lw;
            const unsigned ko = WS_KV + (unsigned)(h * 8192 + lane * 16);
            const unsigned zo = WS_Z + (unsigned)(h * 1024 + i16 * 64 + G * 16);
            u32x4 pv[9];
            unsigned pend = 0x1ff;
#pragma unroll
            for (int j = 0; j < 9; ++j) pv[j] = u32x4{EMPTY, EMPTY, EMPTY, EMPTY};
#pragma nounroll
            for (unsigned spins = 0; spins < SPIN_CAP && pend; ++spins) {
                asm volatile("" ::: "memory");
#pragma unroll
                for (int j = 0; j < 8; ++j) if (pend & (1u << j)) pv[j] = LD128(ko + j * 1024);
                if (pend & 256u) pv[8] = LD128(zo);
                pend = 0;
#pragma unroll
                for (int j = 0; j < 9; ++j) pend |= missing(pv[j]) ? (1u << j) : 0u;
                if (GSLEEP && pend) __builtin_amdgcn_s_sleep(GSLEEP);
            }
            TL(22);
            u32x4 sel;
#pragma unroll
            for (int e = 0; e < 4; ++e) {
                const int k0 = 8 * (G & 1) + 2 * e;
                sel[e] = (k0 == i16 ? 0x3f80u : 0u) | (k0 + 1 == i16 ? 0x3f800000u : 0u);
            }
            f32x4 ks = {0.f, 0.f, 0.f, 0.f};
#pragma unroll
            for (int j = 0; j < 8; ++j) ks = MF32(__builtin_bit_cast(bf16x8, sel), __builtin_bit_cast(bf16x8, pv[j]), ks);
            LAS char* scr = (LAS char*)smem + B_SCR + 8192;
            const f32x4 zf = __builtin_bit_cast(f32x4, pv[8]);
            *(LAS u32x2*)(scr + 2048 + lw * 512 + lane * 8) = pk4(f32x4{row_sum(zf[0]), row_sum(zf[1]), row_sum(zf[2]), row_sum(zf[3])});
            *(LAS u32x2*)(scr + lw * 512 + lane * 8) = pk4(ks);
            TL(20);
            LSTAMP(13);
            LBAR();
            TL(21);
            __builtin_amdgcn_s_sleep(8);
#pragma unroll
            for (int n = 8; n < 16; ++n) OP(n, lds0 + B_WOHI);
            VBAR(0);
            TL(23);
            TLDUMP(1);
        }
        return;
    }

    const int u = w & 3, hf = w >> 2;
    const int h = 4 * g + u;
    LOAD_EPOCH();
    {
        const int lane = fresh_lane();
        CSTAMP(0);
        const u32x4 e4 = {EMPTY, EMPTY, EMPTY, EMPTY};
        const unsigned co = orow + (unsigned)((w * 16 + 2 * g) * 1024 + lane * 16);
        ST128(e4, co); ST128(e4, co + 1024);
        if (br) {
            if (hf) { if (lane < 4) ST128(e4, WS_Z + (unsigned)((h * 16 + r) * 64 + lane * 16)); }
            else if (lane < 32) ST128(e4, WS_KV + (unsigned)((h * 16 + r) * 512 + lane * 16));
        }
    }
    gu64* const myflag = (gu64*)(ws + WS_FLAG + (unsigned)((br * 16 + r) * 8 + g) * FLAG_STRIDE);

    unsigned wo_lo, wo_hi;

    if (br == 0) {
        lcp xr0, xr1, wr0, wr2;
        {
            const int lane = fresh_lane();
            xr0 = L + xread(lane) + hf * A_XH; xr1 = L + (xread(lane) ^ 64u) + hf * A_XH;
            wr0 = L + A_W + trread(lane, u); wr2 = wr0 + 2 * A_WS;
            asm volatile("" : "+v"(wr0), "+v"(wr2));
        }

        f32x4 aq[4], ak[5], av[5];
#pragma unroll
        for (int j = 0; j < 4; ++j) aq[j] = f32x4{0.f, 0.f, 0.f, 0.f};
#pragma unroll
        for (int j = 0; j < 5; ++j) { ak[j] = f32x4{0.f, 0.f, 0.f, 0.f}; av[j] = f32x4{0.f, 0.f, 0.f, 0.f}; }
        bf16x8 xf[2][5], wf[2][3];
#define A_READ(kk) do { const int sl_ = ((kk) >> 1) % 3; const lcp xb_ = (((kk) & 1) ? xr1 : xr0) + sl_ * A_XS; \
        const lcp wb_ = (sl_ == 2 ? wr2 : wr0 + sl_ * A_WS) + ((kk) & 1) * 4096; \
        _Pragma("unroll") for (int s_ = 0; s_ < 3; ++s_) wf[(kk) & 1][s_] = ldsfragT(wb_ + s_ * 8192); \
        _Pragma("unroll") for (int j_ = 0; j_ < 4; ++j_) xf[(kk) & 1][j_ + 1] = lds128(xb_ + j_ * 2048); \
        xf[(kk) & 1][0] = lds128(xb_ + 7168); } while (0)
#define A_MMA(kk) do { \
        _Pragma("unroll") for (int j_ = 0; j_ < 4; ++j_) { \
            aq[j_] = MF32(wf[(kk) & 1][0], xf[(kk) & 1][j_ + 1], aq[j_]); \
            ak[j_ + 1] = MF32(wf[(kk) & 1][1], xf[(kk) & 1][j_ + 1], ak[j_ + 1]); \
            av[j_ + 1] = MF32(xf[(kk) & 1][j_ + 1], wf[(kk) & 1][2], av[j_ + 1]); } \
        ak[0] = MF32(wf[(kk) & 1][1], xf[(kk) & 1][0], ak[0]); \
        av[0] = MF32(xf[(kk) & 1][0], wf[(kk) & 1][2], av[0]); } while (0)

        TL(1);
        BAR();
        TL(2);
        CSTAMP(1);
        A_READ(0);
        SBAR();
#pragma unroll
        for (int t = 0; t < 8; ++t) {
            A_READ(2 * t + 1);
            A_MMA(2 * t);
            WEAVE(6);
            SBAR();
            if (t < 7) {
                TL(2 * t + 3);
                if (t == 3) VLBAR(0); else LBAR();
                TL(2 * t + 4);
                if (t == 3) {
                    asm volatile("" : "+s"(epoch));
                    if (w == 0) __hip_atomic_store(myflag, epoch + 1, RLX_AGENT);
                }
                SBAR();
                A_READ(2 * t + 2);
            }
            A_MMA(2 * t + 1);
            if (t < 7) WEAVE(6);
            SBAR();
        }
        wo_lo = A_WOLO; wo_hi = A_WOHI;
        TL(17);
        CSTAMP(2);

        const int lane = fresh_lane(), i16 = lane & 15, G = lane >> 4;
        f32x4 ccur, cprev, cnone;
        unsigned mcur[2];
#pragma unroll
        for (int e = 0; e < 4; ++e) {
            const int key = 4 * G + e;
            ccur[e] = (key <= i16 && i16 - key <= 7) ? 0.f : -1e30f;
            cprev[e] = (key >= i16 + 9) ? 0.f : -1e30f;
            cnone[e] = (r == 0) ? -1e30f : cprev[e];
        }
#pragma unroll
        for (int e = 0; e < 2; ++e)
            mcur[e] = ((4 * G + 2 * e <= i16) ? 0xffffu : 0u) | ((4 * G + 2 * e + 1 <= i16) ? 0xffff0000u : 0u);
        u32x2 kb[5], vb[5], qb[4], pb[4], ob[4];
#pragma unroll
        for (int j = 0; j < 5; ++j) kb[j] = pk4(ak[j]);
#pragma unroll
        for (int j = 0; j < 4; ++j) qb[j] = pk4(aq[j]);
        const bf16x4 ones = {0x3f80, 0x3f80, 0x3f80, 0x3f80};
        const float cs = 0.25f * 1.4426950408889634f;
        f32x4 sc[4], sp[4];
#pragma unroll
        for (int j = 0; j < 4; ++j) {
            sc[j] = MF16(as4(kb[j + 1]), as4(qb[j]), ccur);
            sp[j] = MF16(as4(kb[j]), as4(qb[j]), j == 0 ? cnone : cprev);
        }
#pragma unroll
        for (int j = 0; j < 5; ++j) vb[j] = pk4(av[j]);
        float mx[4];
#pragma unroll
        for (int j = 0; j < 4; ++j) {
#pragma unroll
            for (int e = 0; e < 4; ++e) sc[j][e] = fmx(sc[j][e], sp[j][e]);
            mx[j] = fmx(fmx(fmx(sc[j][0], sc[j][1]), sc[j][2]), sc[j][3]);
        }
#pragma unroll
        for (int j = 0; j < 4; ++j) mx[j] = -cs * xmax(mx[j]);
        f32x4 sum[4], o[4];
#pragma unroll
        for (int j = 0; j < 4; ++j) {
            f32x4 p;
#pragma unroll
            for (int e = 0; e < 4; ++e) p[e] = __builtin_amdgcn_exp2f(__builtin_fmaf(sc[j][e], cs, mx[j]));
            pb[j] = pk4(p);
            const u32x2 pcur = {pb[j][0] & mcur[0], pb[j][1] & mcur[1]}, pprev = {pb[j][0] & ~mcur[0], pb[j][1] & ~mcur[1]};
            sum[j] = MF16(ones, as4(pb[j]), (f32x4{0.f, 0.f, 0.f, 0.f}));
            o[j] = MF32(as8(vb[j], vb[j + 1]), as8(pprev, pcur), (f32x4{0.f, 0.f, 0.f, 0.f}));
        }
#pragma unroll
        for (int j = 0; j < 4; ++j) {
            const float inv = __builtin_amdgcn_rcpf(sum[j][0]);
            ob[j] = pk4(f32x4{o[j][0] * inv, o[j][1] * inv, o[j][2] * inv, o[j][3] * inv});
        }
#pragma unroll
        for (int jp = 0; jp < 2; ++jp) {
            const u32x4 v = fill4(swap16(ob[2 * jp], ob[2 * jp + 1]));
            const int T = 4 * hf + 2 * jp + (G & 1);
            ST128(v, orow + (unsigned)(((T * 16 + (h >> 1)) * 4 + 2 * (h & 1) + (G >> 1)) * 256 + i16 * 16));
        }
        TL(19);
        CSTAMP(6);
    } else {
        lcp xa[2][4], wr;
        {
            const int lane = fresh_lane();
#pragma unroll
            for (int j = 0; j < 4; ++j) {
                xa[0][j] = L + xread(lane) + (4 * hf + (j ^ u)) * 2048;
                xa[1][j] = L + (xread(lane) ^ 64u) + (4 * hf + (j ^ u)) * 2048;
            }
            wr = L + B_W + trread(lane, u);
            asm volatile("" : "+v"(wr));
        }
        f32x4 ak[4], av[4];
#pragma unroll
        for (int j = 0; j < 4; ++j) { ak[j] = f32x4{1e-6f, 1e-6f, 1e-6f, 1e-6f}; av[j] = f32x4{0.f, 0.f, 0.f, 0.f}; }
        bf16x8 xs[16], xf[2][3], wf[2][2];
#define B_READ(kk) do { const int so_ = (((kk) >> 1) % 3) * 16384; \
        _Pragma("unroll") for (int s_ = 0; s_ < 2; ++s_) wf[(kk) & 1][s_] = ldsfragT(wr + so_ + s_ * 8192 + ((kk) & 1) * 4096); \
        xs[kk] = lds128(xa[(kk) & 1][0] + so_); \
        _Pragma("unroll") for (int j_ = 1; j_ < 4; ++j_) xf[(kk) & 1][j_ - 1] = lds128(xa[(kk) & 1][j_] + so_); } while (0)
#define B_MMA(kk) do { \
        ak[0] = MF32(xs[kk], wf[(kk) & 1][0], ak[0]); \
        _Pragma("unroll") for (int j_ = 1; j_ < 4; ++j_) ak[j_] = MF32(xf[(kk) & 1][j_ - 1], wf[(kk) & 1][0], ak[j_]); \
        if ((kk) == 15) SBAR(); \
        av[0] = MF32(xs[kk], wf[(kk) & 1][1], av[0]); \
        _Pragma("unroll") for (int j_ = 1; j_ < 4; ++j_) av[j_] = MF32(xf[(kk) & 1][j_ - 1], wf[(kk) & 1][1], av[j_]); } while (0)

        TL(1);
        BAR();
        TL(2);
        CSTAMP(1);
        B_READ(0);
        SBAR();
#pragma unroll
        for (int t = 0; t < 8; ++t) {
            B_READ(2 * t + 1);
            B_MMA(2 * t);
            WEAVE(4);
            SBAR();
            if (t < 7) {
                TL(2 * t + 3);
                if (t == 3) VLBAR(0); else LBAR();
                TL(2 * t + 4);
                if (t == 3) {
                    asm volatile("" : "+s"(epoch));
                    if (w == 0) __hip_atomic_store(myflag, epoch + 1, RLX_AGENT);
                }
                SBAR();
                B_READ(2 * t + 2);
            }
            B_MMA(2 * t + 1);
            if (t < 7) WEAVE(4);
            SBAR();
        }
        TL(17);
        CSTAMP(2);
        wo_lo = B_WOLO; wo_hi = B_WOHI;

        const int lane = fresh_lane(), i16 = lane & 15, G = lane >> 4;
        const bf16x8 ones8 = {0x3f80, 0x3f80, 0x3f80, 0x3f80, 0x3f80, 0x3f80, 0x3f80, 0x3f80};
        f32x4 kv = {0.f, 0.f, 0.f, 0.f}, zz = {0.f, 0.f, 0.f, 0.f};
#pragma unroll
        for (int jp = 0; jp < 2; ++jp) {
            const bf16x8 pkf = as8(pk4(phi4(ak[2 * jp])), pk4(phi4(ak[2 * jp + 1])));
            const bf16x8 vf = as8(pk4(av[2 * jp]), pk4(av[2 * jp + 1]));
            kv = MF32(pkf, vf, kv);
            zz = MF32(pkf, ones8, zz);
        }
        LAS char* scr = (LAS char*)smem + B_SCR;
        *(LAS f32x4*)(scr + w * 1024 + lane * 16) = hf ? kv : zz;
        LBAR();
        const f32x4 part = (hf ? zz : kv) + *(const LAS f32x4*)(scr + (w ^ 4) * 1024 + lane * 16);
        if (hf) {
            if (i16 == 0) ST128(fill4(__builtin_bit_cast(u32x4, part)), WS_Z + (unsigned)((h * 16 + r) * 64 + G * 16));
        } else {
            const u32x2 pp = pk4(part);
            ST64((u32x2{fill1(pp[0]), fill1(pp[1])}), WS_KV + (unsigned)((h * 16 + r) * 512 + (G >> 1) * 256 + i16 * 16 + (G & 1) * 8));
        }
        TL(24);
        CSTAMP(3);

        f32x4 aq[4];
#pragma unroll
        for (int c = 0; c < 4; ++c) aq[c] = f32x4{1e-6f, 1e-6f, 1e-6f, 1e-6f};
        lcp qr[4];
#pragma unroll
        for (int c = 0; c < 4; ++c) { qr[c] = L + B_WQ + trread(lane, c); asm volatile("" : "+v"(qr[c])); }
        bf16x8 qf[3][4];
#define Q_READ(kk) do { _Pragma("unroll") for (int c_ = 0; c_ < 4; ++c_) qf[(kk) % 3][c_] = ldsfragT(qr[c_] + (kk) * 4096); } while (0)
        Q_READ(0); Q_READ(1);
        SBAR();
#pragma unroll
        for (int kk = 0; kk < 16; ++kk) {
            if (kk < 14) Q_READ(kk + 2);
#pragma unroll
            for (int c = 0; c < 4; ++c) aq[c] = MF32(qf[kk % 3][c], xs[kk], aq[c]);
            if (kk < 14) WEAVE(4);
            SBAR();
        }
        u32x2 pq[4];
#pragma unroll
        for (int c = 0; c < 4; ++c) { pq[c] = pk4(phi4(aq[c])); asm volatile("" : "+v"(pq[c])); }
        SBAR();
        TL(25);
        CSTAMP(4);
        BAR();
        TL(26);
        CSTAMP(5);
        const lcp scr2 = L + B_SCR + 8192 + lane * 8;
        u32x2 ob[4];
        f32x4 num[4], den[4];
#pragma unroll
        for (int c = 0; c < 4; ++c) {
            const u32x2 kb = *(const LAS u32x2*)(scr2 + c * 512);
            const u32x2 zb = *(const LAS u32x2*)(scr2 + 2048 + c * 512);
            num[c] = MF16(as4(kb), as4(pq[c]), (f32x4{0.f, 0.f, 0.f, 0.f}));
            den[c] = MF16(as4(zb), as4(pq[c]), (f32x4{1e-6f, 1e-6f, 1e-6f, 1e-6f}));
        }
#pragma unroll
        for (int c = 0; c < 4; ++c) {
            const float inv = __builtin_amdgcn_rcpf(den[c][0]);
            ob[c] = pk4(f32x4{num[c][0] * inv, num[c][1] * inv, num[c][2] * inv, num[c][3] * inv});
        }
#pragma unroll
        for (int cp = 0; cp < 2; ++cp) {
            const u32x4 v = fill4(swap16(ob[2 * cp], ob[2 * cp + 1]));
            ST128(v, orow + (unsigned)(((w * 16 + 2 * g + cp) * 4 + 2 * (G & 1) + (G >> 1)) * 256 + i16 * 16));
        }
        TL(19);
        CSTAMP(6);
    }

    phase2(L, wo_lo, wo_hi, rs, orow, w, g, out, (unsigned)((s0 + (w >> 2) + 32 * (w & 3)) * 2048 + br * 1024 + g * 128), ws TLPASS);
    TL(23);
    TLDUMP(0);
    if (w == 0 && br == 1 && r == 0 && g == 0) __hip_atomic_store((gu64*)(ws + ep_off), epoch + 1, RLX_AGENT);
}

extern "C" void kernel_launch(void* const* d_in, const int* in_sizes, int n_in,
                              void* d_out, int out_size, void* d_ws, size_t ws_size,
                              hipStream_t stream) {
    fused_kernel<<<256, 768, 0, stream>>>((const char*)d_in[0], (const char*)d_in[1], (const char*)d_in[2],
                                          (const char*)d_in[3], (const char*)d_in[4], (const char*)d_in[5],
                                          (char*)d_out, (char*)d_ws);
}
```

```cpp
#include <hip/hip_runtime.h>
#include <hip/hip_bf16.h>
#include <stdint.h>

constexpr int CODE_LINES = 212;
constexpr int WOB = 4, WOC = 8;
constexpr int QH = 8;
constexpr int PDA = 7, PDB = 4;
constexpr int TA1 = 3;
constexpr int G_DELAY = 3, GSLEEP = 2;

typedef short bf16x8 __attribute__((ext_vector_type(8)));
typedef short bf16x4 __attribute__((ext_vector_type(4)));
typedef float f32x4 __attribute__((ext_vector_type(4)));
typedef float f32x2 __attribute__((ext_vector_type(2)));
typedef __bf16 bf16v2 __attribute__((ext_vector_type(2)));
typedef unsigned u32x4 __attribute__((ext_vector_type(4)));
typedef unsigned u32x2 __attribute__((ext_vector_type(2)));
typedef unsigned long long u64;
typedef u64 u64x2 __attribute__((ext_vector_type(2)));
#define LAS __attribute__((address_space(3)))
#define GAS __attribute__((address_space(1)))
typedef LAS const char* lcp;
typedef GAS u64 gu64;

constexpr int LDS_BYTES = 163840;
constexpr int A_XS = 18432, A_XH = 9216, A_W = 3 * A_XS, A_WS = 24576, A_WOLO = A_W + 3 * A_WS, A_WOHI = 2 * A_XS, A_OWN = 0;
constexpr int A_SINK = A_WOLO + 32768;
constexpr int B_XS = 16384, B_W = 3 * B_XS, B_WS = 16384, B_WQ = 98304, B_WOLO = 32768, B_WOHI = 65536;
constexpr int B_SCR = 0, B_KVZ = 94208, B_QFL = B_KVZ + 2304, B_SINK = B_KVZ + 2560, B_OWN = 0;

constexpr unsigned WS_EPOCH = 0;
constexpr unsigned WS_FO = 8192;
constexpr unsigned WS_FK = 40960;
constexpr unsigned WS_KV = 65536;
constexpr unsigned WS_Z = WS_KV + 262144;
constexpr unsigned WS_O = 1048576;
constexpr unsigned WS_END = WS_O + 4194304;
constexpr unsigned SPIN_CAP = 1u << 16;
constexpr unsigned EMPTY = 0xffffffffu;

#define RLX_AGENT __ATOMIC_RELAXED, __HIP_MEMORY_SCOPE_AGENT
#define SBAR() __builtin_amdgcn_sched_barrier(0)
#define BAR() asm volatile("s_barrier" ::: "memory")
#define LBAR() asm volatile("s_waitcnt lgkmcnt(0)\n\ts_barrier" ::: "memory")
#define VBAR(N) asm volatile("s_waitcnt vmcnt(" #N ")\n\ts_barrier" ::: "memory")
#define SGB(mask, n) __builtin_amdgcn_sched_group_barrier(mask, n, 0)
#define WEAVE(n) do { _Pragma("unroll") for (int i_ = 0; i_ < (n); ++i_) { SGB(0x008, 1); SGB(0x100, 2); } } while (0)
#define WEAVE1(n) do { _Pragma("unroll") for (int i_ = 0; i_ < (n); ++i_) { SGB(0x008, 1); SGB(0x100, 1); } } while (0)
#define MF32(a, b, c) __builtin_amdgcn_mfma_f32_16x16x32_bf16(a, b, c, 0, 0, 0)
#define MF16(a, b, c) __builtin_amdgcn_mfma_f32_16x16x16bf16_1k(a, b, c, 0, 0, 0)
#define LD128(off) __builtin_amdgcn_raw_buffer_load_b128(rs, off, 0, 16)
#define LD64(off) __builtin_amdgcn_raw_buffer_load_b64(rs, off, 0, 16)
#define ST128(v, off) __builtin_amdgcn_raw_buffer_store_b128(v, rs, off, 0, 16)
#define ST64(v, off) __builtin_amdgcn_raw_buffer_store_b64(v, rs, off, 0, 16)

__device__ __forceinline__ bf16x8 lds128(lcp p) { return *(const LAS bf16x8*)p; }
__device__ __forceinline__ bf16x4 ldstr(lcp p) { return __builtin_amdgcn_ds_read_tr16_b64_v4i16((LAS bf16x4*)p); }
__device__ __forceinline__ bf16x8 ldsfragT(lcp p) {
    bf16x4 a = ldstr(p), b = ldstr(p + 512);
    return __builtin_shufflevector(a, b, 0, 1, 2, 3, 4, 5, 6, 7);
}
__device__ __forceinline__ unsigned pk2(float a, float b) {
    f32x2 v = {a, b};
    return __builtin_bit_cast(unsigned, __builtin_convertvector(v, bf16v2));
}
__device__ __forceinline__ u32x2 pk4(f32x4 v) { return u32x2{pk2(v[0], v[1]), pk2(v[2], v[3])}; }
__device__ __forceinline__ bf16x4 as4(u32x2 v) { return __builtin_bit_cast(bf16x4, v); }
__device__ __forceinline__ bf16x8 as8(u32x2 a, u32x2 b) { return __builtin_bit_cast(bf16x8, u32x4{a[0], a[1], b[0], b[1]}); }
__device__ __forceinline__ u32x4 swap16(u32x2 a, u32x2 b) {
    auto x = __builtin_amdgcn_permlane16_swap(a[0], b[0], false, false);
    auto y = __builtin_amdgcn_permlane16_swap(a[1], b[1], false, false);
    return u32x4{x[0], y[0], x[1], y[1]};
}
__device__ __forceinline__ float fmx(float a, float b) { return __builtin_elementwise_maximum(a, b); }
__device__ __forceinline__ float xmax(float m) {
    auto a = __builtin_amdgcn_permlane16_swap(__float_as_uint(m), __float_as_uint(m), false, false);
    m = fmx(__uint_as_float(a[0]), __uint_as_float(a[1]));
    auto b = __builtin_amdgcn_permlane32_swap(__float_as_uint(m), __float_as_uint(m), false, false);
    return fmx(__uint_as_float(b[0]), __uint_as_float(b[1]));
}
__device__ __forceinline__ void xmax4(float (&m)[4], float scale) {
    auto a = __builtin_amdgcn_permlane16_swap(__float_as_uint(m[0]), __float_as_uint(m[1]), false, false);
    auto b = __builtin_amdgcn_permlane16_swap(__float_as_uint(m[2]), __float_as_uint(m[3]), false, false);
    const float ab = fmx(__uint_as_float(a[0]), __uint_as_float(a[1]));
    const float cd = fmx(__uint_as_float(b[0]), __uint_as_float(b[1]));
    auto c = __builtin_amdgcn_permlane32_swap(__float_as_uint(ab), __float_as_uint(cd), false, false);
    const unsigned all = __float_as_uint(scale * fmx(__uint_as_float(c[0]), __uint_as_float(c[1])));
    auto d = __builtin_amdgcn_permlane32_swap(all, all, false, false);
    auto e = __builtin_amdgcn_permlane16_swap(d[0], d[0], false, false);
    auto f = __builtin_amdgcn_permlane16_swap(d[1], d[1], false, false);
    m[0] = __uint_as_float(e[0]); m[1] = __uint_as_float(e[1]); m[2] = __uint_as_float(f[0]); m[3] = __uint_as_float(f[1]);
}
constexpr float PHI0 = 1.000001f;
__device__ __forceinline__ f32x4 phi4(f32x4 a) {
    const float c = 1.4426950408889634f;
    const f32x4 x = __builtin_elementwise_fma(a, f32x4{c, c, c, c}, f32x4{-c * PHI0, -c * PHI0, -c * PHI0, -c * PHI0});
    f32x4 e = {__builtin_amdgcn_exp2f(x[0]), __builtin_amdgcn_exp2f(x[1]), __builtin_amdgcn_exp2f(x[2]), __builtin_amdgcn_exp2f(x[3])};
    e += f32x4{1e-6f, 1e-6f, 1e-6f, 1e-6f};
    return f32x4{__builtin_amdgcn_fmed3f(e[0], a[0], PHI0), __builtin_amdgcn_fmed3f(e[1], a[1], PHI0),
                 __builtin_amdgcn_fmed3f(e[2], a[2], PHI0), __builtin_amdgcn_fmed3f(e[3], a[3], PHI0)};
}
__device__ __forceinline__ f32x4 row_sum4(f32x4 v) {
    float a, b, c, d;
#define RS1(n, x, y) "v_add_f32_dpp " x ", " y ", " y " row_ror:" #n " row_mask:0xf bank_mask:0xf\n\t"
#define RS4(n) RS1(n, "%0", "%0") RS1(n, "%1", "%1") RS1(n, "%2", "%2") RS1(n, "%3", "%3")
    asm volatile("s_nop 1\n\t" RS1(8, "%0", "%4") RS1(8, "%1", "%5") RS1(8, "%2", "%6") RS1(8, "%3", "%7") RS4(4) RS4(2) RS4(1)
                 : "=&v"(a), "=&v"(b), "=&v"(c), "=&v"(d) : "v"(v[0]), "v"(v[1]), "v"(v[2]), "v"(v[3]));
    return f32x4{a, b, c, d};
}
__device__ __forceinline__ unsigned fill1(unsigned v) { return v < EMPTY - 1u ? v : EMPTY - 1u; }
__device__ __forceinline__ u32x4 fill4(u32x4 v) { return u32x4{fill1(v[0]), fill1(v[1]), fill1(v[2]), fill1(v[3])}; }
__device__ __forceinline__ unsigned umax(unsigned a, unsigned b) { return a > b ? a : b; }
__device__ __forceinline__ bool missing(u32x4 v) {
    return __builtin_amdgcn_ballot_w64(umax(umax(v[0], v[1]), umax(v[2], v[3])) == EMPTY) != 0;
}
__device__ __forceinline__ bool missing2(u32x4 a, u32x4 b) {
    const unsigned m = umax(umax(umax(umax(a[0], a[1]), a[2]), umax(umax(a[3], b[0]), b[1])), umax(b[2], b[3]));
    return __builtin_amdgcn_ballot_w64(m == EMPTY) != 0;
}
__device__ __forceinline__ int fresh_lane() {
    int l;
    asm volatile("v_mbcnt_lo_u32_b32 %0, -1, 0\n\tv_mbcnt_hi_u32_b32 %0, -1, %0" : "=v"(l));
    return l;
}
__device__ __forceinline__ unsigned xread(int lane) {
    const int i16 = lane & 15, G = lane >> 4;
    return (unsigned)(i16 * 128 + ((G ^ (i16 >> 1)) * 16));
}
__device__ __forceinline__ unsigned trread(int lane, int c) {
    const int q4 = (lane & 15) >> 2, G = lane >> 4;
    const int ftr = ((q4 >> 1) & 1) | ((G & 1) << 1);
    return (unsigned)((8 * G + q4) * 128 + (lane & 3) * 8 + ((c ^ ftr) * 32));
}
__device__ __forceinline__ float bflo(unsigned v) { return __uint_as_float(v << 16); }
__device__ __forceinline__ float bfhi(unsigned v) { return __uint_as_float(v & 0xffff0000u); }

__device__ __forceinline__ void oload(u32x4& a, u32x4& b, unsigned voff, __amdgpu_buffer_rsrc_t rs, unsigned soff) {
    asm volatile("buffer_load_dwordx4 %0, %2, %3, %4 offen sc1\n\tbuffer_load_dwordx4 %1, %2, %3, %4 offen offset:1024 sc1"
                 : "=&v"(a), "=&v"(b) : "v"(voff), "s"(rs), "s"(soff) : "memory");
}
#define OWAIT(N, a, b) asm volatile("s_waitcnt vmcnt(" #N ")" : "+v"(a), "+v"(b) :: "memory")

#define FLAGS_OK() __all(fv[0] == tag[0] && fv[1] == tag[1])
#define CHECK_FLAGS(off) do { if (__builtin_expect(!FLAGS_OK(), 0)) { \
        _Pragma("nounroll") for (unsigned spins = 0; spins < SPIN_CAP; ++spins) { \
            __builtin_amdgcn_s_sleep(1); \
            asm volatile("" ::: "memory"); \
            fv = LD64(off); \
            if (FLAGS_OK()) break; } } \
        asm volatile("" ::: "memory"); } while (0)

template <int LO, int HI, int LAST, int OWN, bool SPLIT, int PD>
__device__ __forceinline__ void phase2(lcp L, __amdgpu_buffer_rsrc_t rs, unsigned orow, int w, int g, char* out,
                                       unsigned obase, u32x2 fv, u32x2 tag, unsigned fo) {
    const int lane = fresh_lane(), i16 = lane & 15, G = lane >> 4;
    const unsigned ol = orow + (unsigned)(w * 16384 + lane * 16);
    u32x4 of[16];
#define OLD(j) oload(of[2 * (j)], of[2 * (j) + 1], ol, rs, (unsigned)(((g + (j)) & 7) * 2048))
    lcp wl[4], wh[4];
#pragma unroll
    for (int c = 0; c < 4; ++c) {
        wl[c] = L + LO + trread(lane, c); wh[c] = L + HI + trread(lane, c);
        asm volatile("" : "+v"(wl[c]), "+v"(wh[c]));
    }
    lcp own = L + OWN + w * 2048 + lane * 16;
    asm volatile("" : "+v"(own));
    LBAR();
    bf16x8 wf[4][4];
#define WRD(n) do { _Pragma("unroll") for (int c_ = 0; c_ < 4; ++c_) \
        wf[(n) & 3][c_] = ldsfragT((n) < 8 ? wl[c_] + (n) * 4096 : wh[c_] + ((n) < 15 ? ((n) - 8) * 4096 : LAST - HI)); } while (0)
#define OMM(n) do { _Pragma("unroll") for (int c_ = 0; c_ < 4; ++c_) \
        acc[c_] = MF32(wf[(n) & 3][c_], __builtin_bit_cast(bf16x8, of[n]), acc[c_]); } while (0)
    f32x4 acc[4];
#pragma unroll
    for (int c = 0; c < 4; ++c) acc[c] = f32x4{0.f, 0.f, 0.f, 0.f};
    of[0] = *(const LAS u32x4*)own; of[1] = *(const LAS u32x4*)(own + 1024);
    WRD(0); WRD(1); WRD(2); WRD(3);
    OMM(0); WRD(4); OMM(1); WRD(5);
    SBAR();
    if (SPLIT) {
        asm volatile("s_waitcnt vmcnt(0)" : "+v"(fv) :: "memory");
        CHECK_FLAGS(fo);
    } else {
        asm volatile("s_waitcnt vmcnt(0)" ::: "memory");
    }
#pragma unroll
    for (int j = 1; j <= PD; ++j) OLD(j);
    SBAR();
#define OWAITN(N, a, b) asm volatile("s_waitcnt vmcnt(%2)" : "+v"(a), "+v"(b) : "n"(N) : "memory")
#define PSTEP(j) do { \
        OWAITN(2 * (((j) + PD > 8 ? 8 : (j) + PD) - (j) - 1), of[2 * (j)], of[2 * (j) + 1]); \
        if (__builtin_expect(missing2(of[2 * (j)], of[2 * (j) + 1]), 0)) { \
            _Pragma("nounroll") for (unsigned tries = 0; tries < SPIN_CAP; ++tries) { \
                OLD(j); \
                asm volatile("s_waitcnt vmcnt(2)" ::: "memory"); \
                _Pragma("unroll") for (int k_ = (j) + 1; k_ < 8 && k_ < (j) + PD; ++k_) { \
                    asm volatile("" : "+v"(of[2 * k_]), "+v"(of[2 * k_ + 1]) :: "memory"); \
                    if (missing2(of[2 * k_], of[2 * k_ + 1])) OLD(k_); } \
                OWAIT(0, of[2 * (j)], of[2 * (j) + 1]); \
                if (!missing2(of[2 * (j)], of[2 * (j) + 1])) break; } } \
        if ((j) + PD < 8) OLD((j) + PD); \
        if (SPLIT && (j) == XSTEP) BAR(); \
        SBAR(); \
        OMM(2 * (j)); if ((j) < 6) WRD(2 * (j) + 4); \
        OMM(2 * (j) + 1); if ((j) < 6) WRD(2 * (j) + 5); \
        if ((j) < 6) WEAVE(8); \
        SBAR(); } while (0)
    constexpr int XSTEP = (WOC - 4) / 2;
    PSTEP(1); PSTEP(2); PSTEP(3); PSTEP(4); PSTEP(5); PSTEP(6); PSTEP(7);
    const __amdgpu_buffer_rsrc_t ro = __builtin_amdgcn_make_buffer_rsrc(out, 0, 4194304, 0x00020000);
    const unsigned op = obase + (unsigned)(i16 * 4096 + (G & 1) * 32 + (G >> 1) * 16);
#pragma unroll
    for (int cp = 0; cp < 2; ++cp) {
        const u32x4 v = swap16(pk4(acc[2 * cp]), pk4(acc[2 * cp + 1]));
        __builtin_amdgcn_raw_buffer_store_b128(v, ro, op + cp * 64, 0, 16);
    }
}

__global__ void __launch_bounds__(768, 3)
fused_kernel(const char* xA_, const char* wA_, const char* woA_, const char* xB_, const char* wB_, const char* woB_,
             char* out_, char* ws_) {
    __shared__ __attribute__((aligned(16))) char smem[LDS_BYTES];
    const int b = blockIdx.x, xcd = b & 7, idx = b >> 3;
    const int br = xcd >> 2, rr = idx >> 3, r = (xcd & 3) * 4 + rr, g = idx & 7;
    const int wv = __builtin_amdgcn_readfirstlane(threadIdx.x >> 6);
    unsigned pf = 0;
    if (wv == 11) {
        u64 pc;
        asm volatile("s_getpc_b64 %0" : "=s"(pc));
        constexpr int NL = (CODE_LINES + 31) / 32;
        const int lane = fresh_lane();
        const unsigned ln = (unsigned)(idx * NL + lane);
        if (lane < NL && ln < (unsigned)CODE_LINES)
            asm volatile("global_load_dword %0, %1, %2" : "+v"(pf) : "v"(ln * 128u), "s"(pc & ~255ull) : "memory");
    }
    const int s0 = r * 128;
    const lcp L = (lcp)smem;
    const unsigned ep_off = WS_EPOCH + (unsigned)(br ? 16 : r) * 256u;
    const unsigned orow = WS_O + (unsigned)((br * 16 + r) * 131072);
    u64 epoch;
#define LOAD_EPOCH() asm volatile("s_load_dwordx2 %0, %1, %2" : "=&s"(epoch) : "s"(ws), "s"(ep_off) : "memory")
#define EPOCH_IN() asm volatile("s_waitcnt lgkmcnt(0)" : "+s"(epoch) :: "memory")

    if (__builtin_expect(wv < 4, 1)) {
        u64x2 kxw;
        u64 kwo, kws;
        asm volatile("s_load_dwordx4 %0, %3, %4\n\ts_load_dwordx2 %2, %3, 0x38\n\ts_load_dwordx2 %1, %3, %5"
                     : "=&s"(kxw), "=&s"(kwo), "=&s"(kws)
                     : "s"(__builtin_amdgcn_kernarg_segment_ptr()), "s"(br * 24), "s"(br * 24 + 16) : "memory");
        SBAR();
        const int lane = fresh_lane(), i16 = lane & 15, G = lane >> 4;
        const int lw = wv;
        const unsigned lds0 = (unsigned)(uintptr_t)smem;
        const int pc = lane & 7, rw = lane >> 3;
        unsigned xoff[4];
#pragma unroll
        for (int p = 0; p < 4; ++p)
            xoff[p] = (unsigned)(s0 + 32 * lw + 2 * (8 * (p & 1) + rw) + (p >> 1)) * 1024u + (unsigned)((pc ^ (4 * (p & 1) + G)) * 16);
        const unsigned usw = (unsigned)((((pc >> 1) ^ ((G & 1) | ((lw & 1) << 1))) * 32) + (pc & 1) * 16 + g * 128);
        unsigned woff[6];
#pragma unroll
        for (int p = 0; p < 6; ++p) woff[p] = (unsigned)(8 * lw + rw) * 3072u + usw + (unsigned)((p >> 1) * 1024 + (p & 1) * 98304);
        const int eta = 4 * (lw & 1) + (rw & 3);
        int ht = s0 - 16 + (lw >> 1) + 2 * eta;
        ht = ht < 0 ? 0 : ht;
        unsigned hoff = (unsigned)ht * 1024u + (unsigned)((pc ^ (4 + (eta >> 1))) * 16);
        unsigned hdst = lds0 + (unsigned)((lw >> 1) * A_XH + 8192 + (lw & 1) * 512);
        const int cl = (g + rr) & 3;
        const unsigned ko_ = (cl & 2) ? ((cl & 1) ? 0x67012345 : 0x45670123) : ((cl & 1) ? 0x23456701 : 0x01234567);
        unsigned dx[3], dh[3], dk[3];
#pragma unroll
        for (int s = 0; s < 3; ++s) {
            dx[s] = lds0 + lw * 2048 + s * (br ? B_XS : A_XS);
            dh[s] = dx[s] + (br ? 8192 : A_XH);
            dk[s] = lds0 + lw * 1024 + (br ? B_W + s * B_WS : A_W + 8192 + s * A_WS);
        }
#define KT(t) ((ko_ >> (28 - 4 * (t))) & 7u)
        unsigned xk0 = KT(0) * 128, wk0 = KT(0) * 196608;
        asm volatile("" : "+v"(xoff[0]), "+v"(xoff[1]), "+v"(xoff[2]), "+v"(xoff[3]), "+v"(woff[0]), "+v"(woff[1]), "+v"(woff[2]),
                          "+v"(woff[3]), "+v"(woff[4]), "+v"(woff[5]), "+v"(hoff), "+s"(hdst), "+s"(dx[0]), "+s"(dh[0]), "+s"(dk[0]),
                          "+s"(dx[1]), "+s"(dh[1]), "+s"(dk[1]), "+s"(xk0), "+s"(wk0));
        __builtin_amdgcn_s_setprio(3);
        asm volatile("s_waitcnt lgkmcnt(0)" : "+s"(kxw), "+s"(kwo), "+s"(kws) :: "memory");
        const char* x = (const char*)kxw[0];
        const char* wq = (const char*)kxw[1];
        const char* wo = (const char*)kwo;
        char* ws = (char*)kws;
#define XB(t) (x + ((t) == 0 ? xk0 : KT(t) * 128))
#define WB(t) (wq + ((t) == 0 ? wk0 : KT(t) * 196608))
#define DMA "\n\ts_nop 0\n\tglobal_load_lds_dwordx4 "
#define S_C(t) asm volatile( \
            "s_mov_b32 m0, %10" DMA "%0, %8\n\ts_mov_b32 m0, %12" DMA "%4, %9\n\ts_add_u32 m0, %12, 0x2000" DMA "%6, %9\n\t" \
            "s_add_u32 m0, %10, 0x400" DMA "%1, %8\n\ts_mov_b32 m0, %11" DMA "%2, %8\n\ts_add_u32 m0, %11, 0x400" DMA "%3, %8\n\t" \
            "s_add_u32 m0, %12, 0x1000" DMA "%5, %9\n\ts_add_u32 m0, %12, 0x3000" DMA "%7, %9" \
            :: "v"(xoff[0]), "v"(xoff[1]), "v"(xoff[2]), "v"(xoff[3]), "v"(woff[2]), "v"(woff[3]), "v"(woff[4]), "v"(woff[5]), \
               "s"(XB(t)), "s"(WB(t)), "s"(dx[(t) % 3]), "s"(dh[(t) % 3]), "s"(dk[(t) % 3]) : "memory", "scc")
#define S_X(t, TA, TB) asm volatile( \
            "s_cmp_lg_u32 %8, 0\n\ts_cbranch_scc1 .Lsb%=\n\t" \
            "s_sub_u32 m0, %5, 0x2000" DMA "%0, %4\n\ts_sub_u32 m0, %5, 0x1000" DMA "%1, %4\n\t" \
            "s_add_u32 m0, %6, %7\n\ts_mov_b32 exec_hi, 0\n\ts_nop 0\n\tglobal_load_lds_dwordx4 %2, %3\n\ts_mov_b32 exec_hi, -1\n\t" \
            TA "\n\ts_branch .Lsj%=\n.Lsb%=:\n\t" TB "\n.Lsj%=:" \
            :: "v"(woff[0]), "v"(woff[1]), "v"(hoff), "s"(XB(t)), "s"(WB(t)), "s"(dk[(t) % 3]), "s"(hdst), "n"(((t) % 3) * A_XS), "s"(br) \
            : "memory", "scc")
#define S_XW(t, NA, NB) do { S_X(t, "s_waitcnt vmcnt(" #NA ")", "s_waitcnt vmcnt(" #NB ")"); BAR(); } while (0)
#define WARM_WO(sink) asm volatile("s_mov_b32 m0, %2\n\ts_mov_b32 exec_hi, 0\n\ts_nop 0\n\tglobal_load_lds_dword %0, %1\n\ts_mov_b32 exec_hi, -1" \
                     :: "v"((unsigned)((rr * 128 + lw * 32 + (lane & 31)) * 1024 + g * 128)), "s"(wo), "s"(lds0 + lw * 128 + (sink)) : "memory")
        S_C(0);
        S_X(0, "", "");
        S_C(1);
        LOAD_EPOCH();
        S_XW(1, 11, 8);
        S_C(2); S_XW(2, 11, 8);
        S_C(3);
        EPOCH_IN();
        S_XW(3, 11, 8);
        S_C(4);
        S_XW(4, 11, 8);
        const __amdgpu_buffer_rsrc_t rs = __builtin_amdgcn_make_buffer_rsrc(ws, 0, WS_END, 0x00020000);
        const u32x2 tag = __builtin_bit_cast(u32x2, epoch + 1);
        const unsigned fsel = (lw == 0 && lane == 0) ? 0u : WS_END;
        S_C(5);
        ST64(tag, (br ? fsel : WS_END) + WS_FK + (unsigned)((g * 16 + r) * 128));
        S_XW(5, 12, 9);
        S_C(6); S_XW(6, 11, 8);
        S_C(7);
        const u32x4 e4 = {EMPTY, EMPTY, EMPTY, EMPTY};
        const unsigned co = orow + (unsigned)((lw * 32 + 2 * g) * 1024 + lane * 16);
#define CLEAR_O() do { ST128(e4, co); ST128(e4, co + 1024); ST128(e4, co + 16384); ST128(e4, co + 17408); } while (0)
        S_X(7, "", "s_waitcnt vmcnt(8)");
        if (!br) { CLEAR_O(); WARM_WO(A_SINK); }
        VBAR(16);
        const unsigned ooff = (unsigned)(8 * lw + rw) * 1024u + usw;
        const char* tb = br ? wq : wo;
        const unsigned tv = br ? woff[0] : ooff, ts = br ? 98304u : 32768u;
        const unsigned tt = br ? ko_ : (unsigned)(0x0123456701234567ull >> (32 - 4 * g));
        const unsigned tlo = lds0 + lw * 1024 + (br ? B_WQ : A_WOLO), thi = lds0 + lw * 1024 + (br ? B_WQ + 32768 : A_WOHI);
#define BLK(v, base, d, n) asm volatile("s_add_u32 m0, %2, %3" DMA "%0, %1" :: "v"(v), "s"(base), "s"(d), "n"(n) : "memory", "scc")
#define TP(n) BLK(tv, tb + (2 * ((tt >> (28 - 4 * ((n) >> 1))) & 7u) + ((n) & 1)) * ts, (n) < 8 ? tlo : thi, ((n) & 7) * 4096)
#pragma unroll
        for (int n = 0; n < TA1; ++n) TP(n);
        if (!br) {
            asm volatile("s_waitcnt vmcnt(%0)\n\ts_barrier" :: "n"(TA1 + 1) : "memory");
            ST64(tag, fsel + WS_FO + (unsigned)(r * 1024 + g * 128));
        }
#pragma unroll
        for (int n = TA1; n < 8; ++n) TP(n);
        if (br) { VBAR(8); }
        if (br) {
            CLEAR_O();
        } else {
#pragma unroll
            for (int n = QH; n < 16; ++n) TP(n);
        }
        const unsigned fo = WS_FO + (unsigned)((br * 16 + r) * 1024 + (lane & 7) * 128);
        const unsigned fk = WS_FK + (unsigned)((g * 16 + i16) * 128);
#define FLAG_LD(off) asm volatile("buffer_load_dwordx2 %0, %1, %2, 0 offen sc1" : "=&v"(fv) : "v"(off), "s"(rs) : "memory")
        if (!br) {
#pragma nounroll
            for (unsigned spins = 0; spins < SPIN_CAP; ++spins) {
                u32x2 fv;
                FLAG_LD(fo);
                asm volatile("s_waitcnt vmcnt(0)" : "+v"(fv) :: "memory");
                if (FLAGS_OK()) break;
            }
            BAR();
            ST64(tag, (g == 0 ? fsel : WS_END) + ep_off);
            return;
        }
        {
            u32x2 fv;
            FLAG_LD(fk);
            const unsigned owd = lds0 + lw * 1024;
#define B_OP(n) BLK(wob[n], wo, owd, (n) < 15 ? B_WOLO + (n) * 4096 : B_WQ)
            const int h = 4 * g + lw;
            unsigned ko = WS_KV + (unsigned)(h * 8192 + lane * 16), ko2 = ko + 4096;
            unsigned zo = WS_Z + (unsigned)(h * 1024 + i16 * 64 + G * 16);
            unsigned pfo = (unsigned)((rr * 128 + lw * 32 + (lane & 31)) * 1024 + g * 128), fo_ = (lane == 0 ? 0u : WS_END) + WS_FO + (unsigned)((16 + r) * 1024 + g * 128 + lw * 8);
            u32x4 sel;
#pragma unroll
            for (int e = 0; e < 4; ++e) {
                const int k0 = 8 * (G & 1) + 2 * e;
                sel[e] = (k0 == i16 ? 0x3f80u : 0u) | (k0 + 1 == i16 ? 0x3f800000u : 0u);
            }
            LAS char* scr = (LAS char*)smem + B_KVZ + lw * 512 + lane * 8;
            LAS char* scz = (LAS char*)smem + B_KVZ + 2048 + lw * 32 + G * 8;
            u32x4 pv[9];
            u32x2 fw = tag;
            unsigned wob[16];
#pragma unroll
            for (int n = 0; n < 16; ++n) { wob[n] = ooff + (unsigned)(((n + 2 * g) & 15) * 32768); asm volatile("" : "+v"(wob[n])); }
#pragma unroll
            for (int j = 0; j < 9; ++j) pv[j] = u32x4{0u, 0u, 0u, 0u};
            asm volatile("" : "+v"(sel), "+v"(ko), "+v"(ko2), "+v"(zo), "+v"(pfo), "+v"(fo_), "+v"(scr), "+v"(scz), "+v"(fw),
                              "+v"(pv[0]), "+v"(pv[1]), "+v"(pv[2]), "+v"(pv[3]), "+v"(pv[4]), "+v"(pv[5]), "+v"(pv[6]), "+v"(pv[7]), "+v"(pv[8]));
            BAR();
            VBAR(5);
#pragma unroll
            for (int n = QH; n < 16; ++n) TP(n);
            asm volatile("s_waitcnt vmcnt(0)\n\ts_barrier" : "+v"(fv) :: "memory");
            CHECK_FLAGS(fk);
            __builtin_amdgcn_s_sleep(G_DELAY);
            unsigned pend = 0x5ff;
#define WB1(imm, op) "s_add_u32 m0, %19, " #imm DMA #op ", %17\n\t"
#define WOBS WB1(0x8000, %21) WB1(0x9000, %22) WB1(0xa000, %23) WB1(0xb000, %24)
            static_assert(B_WOLO == 0x8000 && WOB == 4 && WOC >= 6 && WOC <= 15 && QH == 8, "staging");
#define GLD(j, ins) "s_bitcmp0_b32 %16, " #j "\n\ts_cbranch_scc1 .Lg" #j "_%=\n\t" ins "\n.Lg" #j "_%=:\n\t"
#pragma nounroll
            for (unsigned spins = 0; spins < SPIN_CAP; ++spins) {
                asm volatile(GLD(0, "buffer_load_dwordx4 %0, %10, %15, 0 offen sc1")
                             GLD(1, "buffer_load_dwordx4 %1, %10, %15, 0 offen offset:1024 sc1")
                             GLD(2, "buffer_load_dwordx4 %2, %10, %15, 0 offen offset:2048 sc1")
                             GLD(3, "buffer_load_dwordx4 %3, %10, %15, 0 offen offset:3072 sc1")
                             GLD(4, "buffer_load_dwordx4 %4, %11, %15, 0 offen sc1")
                             GLD(5, "buffer_load_dwordx4 %5, %11, %15, 0 offen offset:1024 sc1")
                             GLD(6, "buffer_load_dwordx4 %6, %11, %15, 0 offen offset:2048 sc1")
                             GLD(7, "buffer_load_dwordx4 %7, %11, %15, 0 offen offset:3072 sc1")
                             GLD(8, "buffer_load_dwordx4 %8, %12, %15, 0 offen sc1")
                             GLD(10, "buffer_store_dwordx2 %9, %13, %15, 0 offen sc1\n\ts_mov_b32 m0, %18\n\ts_mov_b32 exec_hi, 0\n\ts_nop 0\n\tglobal_load_lds_dword %14, %17\n\t"
                                     "s_mov_b32 exec_hi, -1\n\t" WOBS "s_waitcnt vmcnt(%20)\n\ts_branch .Lge%=")
                             "s_waitcnt vmcnt(0)\n.Lge%=:"
                             : "+v"(pv[0]), "+v"(pv[1]), "+v"(pv[2]), "+v"(pv[3]), "+v"(pv[4]), "+v"(pv[5]), "+v"(pv[6]), "+v"(pv[7]),
                               "+v"(pv[8]), "+v"(fw)
                             : "v"(ko), "v"(ko2), "v"(zo), "v"(fo_), "v"(pfo), "s"(rs), "s"(pend), "s"(wo), "s"(lds0 + lw * 128 + B_SINK),
                               "s"(owd), "n"(WOB + 9), "v"(wob[0]), "v"(wob[1]), "v"(wob[2]), "v"(wob[3])
                             : "memory", "scc");
                f32x4 ks = {0.f, 0.f, 0.f, 0.f}, kt = {0.f, 0.f, 0.f, 0.f};
#pragma unroll
                for (int j = 0; j < 4; ++j) {
                    asm volatile("s_waitcnt vmcnt(%2)" : "+v"(pv[2 * j]), "+v"(pv[2 * j + 1]) : "n"(WOB + 9 - 2 * j) : "memory");
                    ks = MF32(__builtin_bit_cast(bf16x8, sel), __builtin_bit_cast(bf16x8, pv[2 * j]), ks);
                    kt = MF32(__builtin_bit_cast(bf16x8, sel), __builtin_bit_cast(bf16x8, pv[2 * j + 1]), kt);
                    SBAR();
                }
                asm volatile("s_waitcnt vmcnt(%1)" : "+v"(pv[8]) : "n"(WOB + 2) : "memory");
                const f32x4 zs = row_sum4(__builtin_bit_cast(f32x4, pv[8]));
                if (i16 == 0) *(LAS u32x2*)scz = pk4(zs);
                ks += kt;
                *(LAS u32x2*)scr = pk4(ks);
                if (!__any(__builtin_isunordered(ks[0], zs[0] + zs[1]) || __builtin_isunordered(zs[2], zs[3]))) break;
                pend = 0u;
#pragma unroll
                for (int j = 0; j < 9; ++j) pend |= missing(pv[j]) ? (1u << j) : 0u;
                if (!pend) break;
                __builtin_amdgcn_s_sleep(GSLEEP);
            }
#pragma unroll
            for (int n = WOB; n < WOC; ++n) B_OP(n);
            LBAR();
            __builtin_amdgcn_s_setprio(0);
            VBAR(0);
#pragma unroll
            for (int n = WOC; n < 16; ++n) B_OP(n);
            VBAR(0);
        }
        return;
    }

    const int w = wv - 4, u = w & 3, hf = w >> 2;
    const int h = 4 * g + u;
    u64x2 kow;
    asm volatile("s_load_dwordx4 %0, %1, 0x30\n\ts_waitcnt lgkmcnt(0)" : "=&s"(kow) : "s"(__builtin_amdgcn_kernarg_segment_ptr()) : "memory");
    char* const ws = (char*)kow[1];
    char* const out = (char*)kow[0];
    const __amdgpu_buffer_rsrc_t rs = __builtin_amdgcn_make_buffer_rsrc(ws, 0, WS_END, 0x00020000);
#define DRAIN_BAR() asm volatile("s_waitcnt vmcnt(0) lgkmcnt(0)\n\ts_barrier" : "+v"(pf) :: "memory")
    const unsigned obase = (unsigned)((s0 + (w >> 2) + 32 * (w & 3)) * 2048 + br * 1024 + g * 128);

    if (br) {
        const int kh = u & 1;
        lcp xa[2][3], wr[2];
        LOAD_EPOCH();
        {
            const int lane = fresh_lane();
            const u32x4 e4 = {EMPTY, EMPTY, EMPTY, EMPTY};
            if (hf) { if (lane < 4) ST128(e4, WS_Z + (unsigned)((h * 16 + r) * 64 + lane * 16)); }
            else if (lane < 32) ST128(e4, WS_KV + (unsigned)((h * 16 + r) * 512 + lane * 16));
#pragma unroll
            for (int e = 0; e < 2; ++e) {
                const lcp xb = L + (xread(lane) ^ (unsigned)((e ^ kh) * 64)) + hf * 8192;
                xa[e][0] = xb + u * 2048; xa[e][1] = xb + (u ^ 1) * 2048; xa[e][2] = xb + ((u & 2) ^ 2) * 2048;
                wr[e] = L + B_W + trread(lane, u) + (e ^ kh) * 4096;
                asm volatile("" : "+v"(wr[e]));
            }
        }
        f32x4 ak[4], av[4];
#pragma unroll
        for (int j = 0; j < 4; ++j) { ak[j] = f32x4{PHI0, PHI0, PHI0, PHI0}; av[j] = f32x4{0.f, 0.f, 0.f, 0.f}; }
        bf16x8 xs[8][2], xe[2], xo[4], wf[2][2];
#define B_X(kk, j) (((kk) & 1) ? xo[j] : ((j) < 2 ? xs[(kk) >> 1][(j) & 1] : xe[(j) & 1]))
#define B_READ(kk) do { const int so_ = (((kk) >> 1) % 3) * 16384; \
        _Pragma("unroll") for (int s_ = 0; s_ < 2; ++s_) wf[(kk) & 1][s_] = ldsfragT(wr[(kk) & 1] + so_ + s_ * 8192); \
        _Pragma("unroll") for (int j_ = 0; j_ < 4; ++j_) B_X(kk, j_) = lds128(xa[(kk) & 1][j_ < 2 ? j_ : 2] + so_ + (j_ == 3 ? 2048 : 0)); } while (0)
#define B_MMA(kk) do { \
        _Pragma("unroll") for (int j_ = 0; j_ < 4; ++j_) ak[j_] = MF32(B_X(kk, j_), wf[(kk) & 1][0], ak[j_]); \
        _Pragma("unroll") for (int j_ = 0; j_ < 4; ++j_) av[j_] = MF32(B_X(kk, j_), wf[(kk) & 1][1], av[j_]); } while (0)

        BAR();
        B_READ(0);
        SBAR();
#pragma unroll
        for (int t = 0; t < 7; ++t) {
            B_READ(2 * t + 1);
            B_MMA(2 * t);
            WEAVE(4);
            SBAR();
            if (t == 2) DRAIN_BAR(); else LBAR();
            if (t == 0) asm volatile("" : "+s"(epoch));
            if (t == 6) *(LAS unsigned*)((LAS char*)smem + B_QFL + w * 4) = 0u;
            SBAR();
            B_READ(2 * t + 2);
            B_MMA(2 * t + 1);
            WEAVE(4);
            SBAR();
        }
#define B_K(kk, j) ak[j] = MF32(B_X(kk, j), wf[(kk) & 1][0], ak[j])
#define B_V(kk, j) av[j] = MF32(B_X(kk, j), wf[(kk) & 1][1], av[j])
        B_READ(15);
        B_K(14, 0); B_K(14, 1); B_K(14, 2); B_K(14, 3);
        WEAVE(4);
        SBAR();
        B_V(14, 0); B_V(14, 1);
        SBAR();
        B_K(15, 0); SBAR(); B_K(15, 1); SBAR(); B_K(15, 2); SBAR(); B_K(15, 3);
        SBAR();

        const int lane = fresh_lane(), i16 = lane & 15, G = lane >> 4;
        const bf16x8 ones8 = {0x3f80, 0x3f80, 0x3f80, 0x3f80, 0x3f80, 0x3f80, 0x3f80, 0x3f80};
        f32x4 kv = {0.f, 0.f, 0.f, 0.f}, zz = {0.f, 0.f, 0.f, 0.f};
        u32x2 pk_[4];
        B_V(15, 0); SBAR(); pk_[0] = pk4(phi4(ak[0])); B_V(15, 1); SBAR();
        B_V(14, 2); SBAR(); pk_[1] = pk4(phi4(ak[1])); B_V(14, 3); SBAR();
        B_V(15, 2); SBAR(); B_V(15, 3);
        {
            const bf16x8 vf = as8(pk4(av[0]), pk4(av[1]));
            zz = MF32(as8(pk_[0], pk_[1]), ones8, zz);
            kv = MF32(as8(pk_[0], pk_[1]), vf, kv);
        }
        pk_[2] = pk4(phi4(ak[2])); SBAR();
        {
            const bf16x8 vf = as8(pk4(av[2]), pk4(av[3]));
            pk_[3] = pk4(phi4(ak[3]));
            kv = MF32(as8(pk_[2], pk_[3]), vf, kv);
            zz = MF32(as8(pk_[2], pk_[3]), ones8, zz);
        }
        LAS char* scr = (LAS char*)smem + B_SCR + lane * 16;
        *(LAS f32x4*)(scr + (w ^ 4) * 1024) = hf ? kv : zz;
        LBAR();
        const f32x4 part = (hf ? zz : kv) + *(const LAS f32x4*)(scr + w * 1024);
        if (hf) {
            if (i16 == 0) ST128(fill4(__builtin_bit_cast(u32x4, part)), WS_Z + (unsigned)((h * 16 + r) * 64 + G * 16));
        } else {
            const u32x2 pp = pk4(part);
            ST64((u32x2{fill1(pp[0]), fill1(pp[1])}), WS_KV + (unsigned)((h * 16 + r) * 512 + (G >> 1) * 256 + i16 * 16 + (G & 1) * 8));
        }
        BAR();
        const unsigned oslot = orow + (unsigned)(((w * 16 + 2 * g) * 4 + 2 * (G & 1) + (G >> 1)) * 256 + i16 * 16);

        f32x4 aq[2][4];
#pragma unroll
        for (int c = 0; c < 4; ++c) { aq[0][c] = f32x4{PHI0, PHI0, PHI0, PHI0}; aq[1][c] = f32x4{0.f, 0.f, 0.f, 0.f}; }
        lcp qr[4];
#pragma unroll
        for (int c = 0; c < 4; ++c) { qr[c] = L + B_WQ + trread(lane, c) + kh * 4096; asm volatile("" : "+v"(qr[c])); }
        bf16x8 qf[2][4];
#define Q_READ(t) do { _Pragma("unroll") for (int c_ = 0; c_ < 4; ++c_) qf[(t) & 1][c_] = ldsfragT(qr[c_] + (t) * 8192); } while (0)
        Q_READ(0);
        SBAR();
#define Q_MMA(t, e) do { _Pragma("unroll") for (int c_ = 0; c_ < 4; ++c_) aq[e][c_] = MF32(qf[(t) & 1][c_], xs[t][e], aq[e][c_]); } while (0)
#pragma unroll
        for (int t = 0; t < 6; ++t) {
            if (t == QH / 2 - 1) { BAR(); SBAR(); }
            Q_READ(t + 1);
            Q_MMA(t, 1);
            Q_MMA(t, 0);
            WEAVE1(8);
            SBAR();
        }
        Q_READ(7);
        Q_MMA(6, 1);
        WEAVE(4);
        SBAR();
        aq[0][0] = MF32(qf[0][0], xs[6][0], aq[0][0]);
        aq[0][1] = MF32(qf[0][1], xs[6][0], aq[0][1]);
        SBAR();
        Q_MMA(7, 1);
        SBAR();
        aq[0][2] = MF32(qf[0][2], xs[6][0], aq[0][2]);
        aq[0][3] = MF32(qf[0][3], xs[6][0], aq[0][3]);
        SBAR();
#pragma unroll
        for (int c = 0; c < 4; ++c) *(LAS f32x4*)(scr + c * 8192 + w * 1024) = aq[1][c];
        volatile LAS unsigned* qfl = (volatile LAS unsigned*)((LAS char*)smem + B_QFL);
        asm volatile("" ::: "memory");
        qfl[w] = 1u;
        SBAR();
        Q_MMA(7, 0);
        SBAR();
#pragma nounroll
        for (unsigned spins = 0; spins < SPIN_CAP && __builtin_amdgcn_readfirstlane(qfl[w ^ 1]) == 0u; ++spins) {}
        asm volatile("" ::: "memory");
        u32x2 pq[4];
#pragma unroll
        for (int c = 0; c < 4; ++c) {
            pq[c] = pk4(phi4(aq[0][c] + *(const LAS f32x4*)(scr + c * 8192 + (w ^ 1) * 1024)));
            asm volatile("" : "+v"(pq[c]));
        }
        SBAR();
        const u32x2 tag = __builtin_bit_cast(u32x2, epoch + 1);
        unsigned fo = WS_FO + (unsigned)((16 + r) * 1024 + (lane & 7) * 128 + (w >> 1) * 8);
        asm volatile("" : "+v"(fo));
        BAR();
        u32x2 fv;
        asm volatile("buffer_load_dwordx2 %0, %1, %2, 0 offen sc1" : "=&v"(fv) : "v"(fo), "s"(rs) : "memory");
        const lcp scr2 = L + B_KVZ;
        u32x2 ob[4];
        f32x4 num[4], den[4];
#pragma unroll
        for (int c = 0; c < 4; ++c) {
            const u32x2 kb = *(const LAS u32x2*)(scr2 + c * 512 + lane * 8);
            const u32x2 zb = *(const LAS u32x2*)(scr2 + 2048 + c * 32 + G * 8);
            num[c] = MF16(as4(kb), as4(pq[c]), (f32x4{0.f, 0.f, 0.f, 0.f}));
            den[c] = MF16(as4(zb), as4(pq[c]), (f32x4{1e-6f, 1e-6f, 1e-6f, 1e-6f}));
        }
#pragma unroll
        for (int c = 0; c < 4; ++c) {
            const float inv = __builtin_amdgcn_rcpf(den[c][0]);
            ob[c] = pk4(f32x4{num[c][0] * inv, num[c][1] * inv, num[c][2] * inv, num[c][3] * inv});
        }
#pragma unroll
        for (int cp = 0; cp < 2; ++cp) {
            const u32x4 v = fill4(swap16(ob[2 * cp], ob[2 * cp + 1]));
            ST128(v, oslot + cp * 1024);
            *(LAS u32x4*)((LAS char*)smem + B_OWN + w * 2048 + cp * 1024 + (2 * (G & 1) + (G >> 1)) * 256 + i16 * 16) = v;
        }
        phase2<B_WOLO, B_WOHI, B_WQ, B_OWN, true, PDB>(L, rs, orow, w, g, out, obase, fv, tag, fo);
        if (w == 0 && r == 0 && g == 0 && fresh_lane() == 0) ST64(tag, ep_off);
    } else {
        lcp xr0, xr1, wr0, wr2;
        f32x4 ccur, cprev, cnone;
        unsigned mcur[2], oslot;
        LAS char* oown;
        {
            const int lane = fresh_lane(), i16 = lane & 15, G = lane >> 4;
            xr0 = L + xread(lane) + hf * A_XH; xr1 = L + (xread(lane) ^ 64u) + hf * A_XH;
            wr0 = L + A_W + trread(lane, u); wr2 = wr0 + 2 * A_WS;
            asm volatile("" : "+v"(wr0), "+v"(wr2));
#pragma unroll
            for (int e = 0; e < 4; ++e) {
                const int key = 4 * G + e;
                ccur[e] = (key <= i16 && i16 - key <= 7) ? 0.f : -1e30f;
                cprev[e] = (key >= i16 + 9) ? 0.f : -1e30f;
                cnone[e] = (r == 0) ? -1e30f : cprev[e];
            }
#pragma unroll
            for (int e = 0; e < 2; ++e)
                mcur[e] = ((4 * G + 2 * e <= i16) ? 0xffffu : 0u) | ((4 * G + 2 * e + 1 <= i16) ? 0xffff0000u : 0u);
            oslot = orow + (unsigned)((((4 * hf + (G & 1)) * 16 + (h >> 1)) * 4 + 2 * (h & 1) + (G >> 1)) * 256 + i16 * 16);
            oown = (LAS char*)smem + A_OWN + (4 * hf + (G & 1)) * 2048 + (2 * u + (G >> 1)) * 256 + i16 * 16;
            asm volatile("" : "+v"(ccur), "+v"(cprev), "+v"(cnone), "+v"(mcur[0]), "+v"(mcur[1]), "+v"(oslot), "+v"(oown));
        }

        f32x4 aq[4], ak[5], av[5];
#pragma unroll
        for (int j = 0; j < 4; ++j) aq[j] = f32x4{0.f, 0.f, 0.f, 0.f};
#pragma unroll
        for (int j = 0; j < 5; ++j) { ak[j] = f32x4{0.f, 0.f, 0.f, 0.f}; av[j] = f32x4{0.f, 0.f, 0.f, 0.f}; }
        bf16x8 xf[2][5], wf[2][3];
#define A_READ(kk) do { const int sl_ = ((kk) >> 1) % 3; const lcp xb_ = (((kk) & 1) ? xr1 : xr0) + sl_ * A_XS; \
        const lcp wb_ = (sl_ == 2 ? wr2 : wr0 + sl_ * A_WS) + ((kk) & 1) * 4096; \
        _Pragma("unroll") for (int s_ = 0; s_ < 3; ++s_) wf[(kk) & 1][s_] = ldsfragT(wb_ + s_ * 8192); \
        _Pragma("unroll") for (int j_ = 0; j_ < 4; ++j_) xf[(kk) & 1][j_ + 1] = lds128(xb_ + j_ * 2048); \
        xf[(kk) & 1][0] = lds128(xb_ + 7168); } while (0)
#define A_MMA(kk) do { \
        _Pragma("unroll") for (int j_ = 0; j_ < 4; ++j_) { \
            aq[j_] = MF32(wf[(kk) & 1][0], xf[(kk) & 1][j_ + 1], aq[j_]); \
            ak[j_ + 1] = MF32(wf[(kk) & 1][1], xf[(kk) & 1][j_ + 1], ak[j_ + 1]); \
            av[j_ + 1] = MF32(xf[(kk) & 1][j_ + 1], wf[(kk) & 1][2], av[j_ + 1]); } \
        ak[0] = MF32(wf[(kk) & 1][1], xf[(kk) & 1][0], ak[0]); \
        av[0] = MF32(xf[(kk) & 1][0], wf[(kk) & 1][2], av[0]); } while (0)
#define A_Q(e, j) aq[j] = MF32(wf[e][0], xf[e][(j) + 1], aq[j])
#define A_K(e, j) ak[j] = MF32(wf[e][1], xf[e][j], ak[j])
#define A_V(e, j) av[j] = MF32(xf[e][j], wf[e][2], av[j])

        BAR();
        A_READ(0);
        SBAR();
#pragma unroll
        for (int t = 0; t < 7; ++t) {
            A_READ(2 * t + 1);
            A_MMA(2 * t);
            WEAVE(6);
            SBAR();
            if (t == 2) DRAIN_BAR(); else LBAR();
            SBAR();
            A_READ(2 * t + 2);
            A_MMA(2 * t + 1);
            WEAVE(6);
            SBAR();
        }
        A_READ(15);
#pragma unroll
        for (int j = 0; j < 4; ++j) { A_Q(0, j); A_K(0, j + 1); }
        A_K(0, 0);
        WEAVE(6);
        SBAR();
        A_K(1, 1); A_Q(1, 0); A_K(1, 0); A_K(1, 2); A_Q(1, 1);
        SBAR();

        u32x2 kb[5], vb[5], qb[4], pb[4], ob[4];
        f32x4 sc[4], sp[4], sum[4], o[4];
        float mx[4];
        const bf16x4 ones = {0x3f80, 0x3f80, 0x3f80, 0x3f80};
        const float cs = 0.25f * 1.4426950408889634f;
#define A_S(j) do { sc[j] = MF16(as4(kb[(j) + 1]), as4(qb[j]), ccur); sp[j] = MF16(as4(kb[j]), as4(qb[j]), (j) == 0 ? cnone : cprev); } while (0)
#define A_M(j) do { _Pragma("unroll") for (int e_ = 0; e_ < 4; ++e_) sc[j][e_] = fmx(sc[j][e_], sp[j][e_]); \
        mx[j] = fmx(fmx(fmx(sc[j][0], sc[j][1]), sc[j][2]), sc[j][3]); } while (0)
#define A_P(j) do { f32x4 p_; \
        _Pragma("unroll") for (int e_ = 0; e_ < 4; ++e_) p_[e_] = __builtin_amdgcn_exp2f(__builtin_fmaf(sc[j][e_], cs, mx[j])); \
        pb[j] = pk4(p_); } while (0)
#define A_O(j) do { const u32x2 pc_ = {pb[j][0] & mcur[0], pb[j][1] & mcur[1]}, pp_ = {pb[j][0] & ~mcur[0], pb[j][1] & ~mcur[1]}; \
        sum[j] = MF16(ones, as4(pb[j]), (f32x4{0.f, 0.f, 0.f, 0.f})); \
        o[j] = MF32(as8(vb[j], vb[(j) + 1]), as8(pp_, pc_), (f32x4{0.f, 0.f, 0.f, 0.f})); } while (0)
#define A_N(j) do { const float inv_ = __builtin_amdgcn_rcpf(sum[j][0]); \
        ob[j] = pk4(f32x4{o[j][0] * inv_, o[j][1] * inv_, o[j][2] * inv_, o[j][3] * inv_}); } while (0)
#define A_ST(jp) do { const u32x4 v_ = fill4(swap16(ob[2 * (jp)], ob[2 * (jp) + 1])); \
        ST128(v_, oslot + (jp) * 32768); *(LAS u32x4*)(oown + (jp) * 4096) = v_; } while (0)
        A_K(1, 3); kb[1] = pk4(ak[1]); qb[0] = pk4(aq[0]); SBAR();
        A_Q(1, 2); kb[0] = pk4(ak[0]); kb[2] = pk4(ak[2]); SBAR();
        A_K(1, 4); A_S(0); qb[1] = pk4(aq[1]); SBAR();
        A_Q(1, 3); A_S(1); kb[3] = pk4(ak[3]); qb[2] = pk4(aq[2]); SBAR();
        A_V(0, 0); A_S(2); kb[4] = pk4(ak[4]); qb[3] = pk4(aq[3]); SBAR();
        A_V(1, 0); A_S(3); A_M(0); SBAR();
        A_V(0, 1); A_M(1); SBAR();
        A_V(1, 1); A_M(2); SBAR();
        A_V(0, 2); A_M(3); SBAR();
        A_V(1, 2); xmax4(mx, -cs); SBAR();
        A_V(0, 3); A_P(0); vb[0] = pk4(av[0]); vb[1] = pk4(av[1]); SBAR();
        A_V(1, 3); A_O(0); A_P(1); vb[2] = pk4(av[2]); SBAR();
        A_V(0, 4); A_O(1); A_P(2); SBAR();
        A_V(1, 4); A_N(0); vb[3] = pk4(av[3]); SBAR();
        A_O(2); A_P(3); A_N(1); vb[4] = pk4(av[4]); SBAR();
        A_O(3); A_ST(0); A_N(2); SBAR();
        A_N(3); A_ST(1);
        phase2<A_WOLO, A_WOHI, A_WOHI + 7 * 4096, A_OWN, false, PDA>(L, rs, orow, w, g, out, obase, u32x2{0u, 0u}, u32x2{0u, 0u}, 0u);
    }
}

extern "C" void kernel_launch(void* const* d_in, const int* in_sizes, int n_in,
                              void* d_out, int out_size, void* d_ws, size_t ws_size,
                              hipStream_t stream) {
    fused_kernel<<<256, 768, 0, stream>>>((const char*)d_in[0], (const char*)d_in[1], (const char*)d_in[2],
                                          (const char*)d_in[3], (const char*)d_in[4], (const char*)d_in[5],
                                          (char*)d_out, (char*)d_ws);
}
```
